```python
import torch

def _softmax(x, axis=-1):
    return torch.softmax(x, dim=axis)
def _gelu(x):
    return torch.nn.functional.gelu(x, approximate="tanh")
def _silu(x):
    return torch.nn.functional.silu(x)
def _sigmoid(x):
    return torch.sigmoid(x)
def _layernorm(x, axis=-1, eps=1e-5):
    m = x.mean(dim=axis, keepdim=True)
    v = x.var(dim=axis, keepdim=True, unbiased=False)
    return (x - m) / torch.sqrt(v + eps)
def _scatter_add(tgt, idx, src, axis=0):
    return tgt.clone().index_add_(axis, idx, src)
def _segment_sum(src, seg, n):
    out = torch.zeros((n,) + src.shape[1:], dtype=src.dtype)
    return out.index_add_(0, seg, src)
def _pad(x, pw):
    flat = tuple(v for pair in reversed(pw) for v in pair)
    return torch.nn.functional.pad(x, flat)

kernel_name = 'sequential_preLN__gqa_block_cached__geglu_ffn__decode'

def setup_inputs():

    _w = (lambda a: (lambda t: t.to(torch.bfloat16) if t.is_floating_point() else t)(torch.as_tensor(a)))
    def _A_setup():
        B, Sq, D = 1, 1, 448
        Hq, Hkv, Sk, Dh = 16, 8, 8192, 256
        assert Hq % Hkv == 0, (Hq, Hkv)
        return {
            "x": _w((lambda *s: torch.randn(*s, dtype=torch.bfloat16) * 0.05)(B, Sq, D)),
            "k_cache": _w((lambda *s: torch.randn(*s, dtype=torch.bfloat16) * 3.2)(B, Hkv, Sk, Dh)),
            "v_cache": _w((lambda *s: torch.randn(*s, dtype=torch.bfloat16) * 0.4)(B, Hkv, Sk, Dh)),
            "wq": _w((lambda *s: torch.randn(*s, dtype=torch.bfloat16) * 3.2)(D, Hq * Dh) * (D ** -0.5)),
            "wo": _w((lambda *s: torch.randn(*s, dtype=torch.bfloat16) * 0.4)(Hq * Dh, D) * ((Hq * Dh) ** -0.5)),
        }
    _A_in = _A_setup()

    def _B_setup():
        B, S, D, F = 1, 1, 448, 1792
        return {
            "x": _w((lambda *s: torch.randn(*s, dtype=torch.bfloat16) * 0.05)(B, S, D)),
            "w_gate": _w((lambda *s: torch.randn(*s, dtype=torch.bfloat16) * 0.05)(D, F)),
            "w_up": _w((lambda *s: torch.randn(*s, dtype=torch.bfloat16) * 0.05)(D, F)),
            "w_down": _w((lambda *s: torch.randn(*s, dtype=torch.bfloat16) * 0.05)(F, D)),
        }
    _B_in = _B_setup()

    return {
        "A_x": _A_in["x"],
        "A_k_cache": _A_in["k_cache"],
        "A_v_cache": _A_in["v_cache"],
        "A_wq": _A_in["wq"],
        "A_wo": _A_in["wo"],
        "B_x": _B_in["x"],
        "B_w_gate": _B_in["w_gate"],
        "B_w_up": _B_in["w_up"],
        "B_w_down": _B_in["w_down"],
    }

def reference(A_x, A_k_cache, A_v_cache, A_wq, A_wo, B_x, B_w_gate, B_w_up, B_w_down):
    def _A_ref(x, k_cache, v_cache, wq, wo):
        B, Sq, D = x.shape
        Hq, Dh = 16, 256
        Hkv = k_cache.shape[1]
        rep = Hq // Hkv
        q = (lambda x, *d: x.permute(*d))((x @ wq).reshape(B, Sq, Hq, Dh), 0, 2, 1, 3)
        k = (lambda x, n, axis: torch.repeat_interleave(x, n, dim=axis))(k_cache, rep, axis=1)
        v = (lambda x, n, axis: torch.repeat_interleave(x, n, dim=axis))(v_cache, rep, axis=1)
        scores = torch.einsum("bhqd,bhkd->bhqk", q, k) * (Dh ** -0.5)
        p = _softmax(scores, axis=-1)
        o = torch.einsum("bhqk,bhkd->bhqd", p, v)
        return (lambda x, *d: x.permute(*d))(o, 0, 2, 1, 3).reshape(B, Sq, Hq * Dh) @ wo

    def _B_ref(x, w_gate, w_up, w_down):
        return (_gelu(x @ w_gate) * (x @ w_up)) @ w_down

    x = A_x
    h = x + _A_ref(_layernorm(x, axis=-1), A_k_cache, A_v_cache, A_wq, A_wo)
    h2 = h + _B_ref(_layernorm(h, axis=-1), B_w_gate, B_w_up, B_w_down)
    return h2
```

```cpp
#include <hip/hip_runtime.h>
#include <hip/hip_bf16.h>
#include <stdint.h>

#define MIMIC 0

typedef unsigned short u16;
typedef unsigned v4u __attribute__((ext_vector_type(4)));

__device__ __forceinline__ float bf2f(unsigned b) { return __builtin_bit_cast(float, b << 16); }
__device__ __forceinline__ unsigned f2bf(float f) {
  unsigned u = __builtin_bit_cast(unsigned, f);
  return (u + 0x7fffu + ((u >> 16) & 1u)) >> 16;
}
__device__ __forceinline__ float rbf(float f) { return bf2f(f2bf(f)); }

__device__ __forceinline__ float wave_sum(float v) {
  for (int o = 32; o >= 1; o >>= 1) v += __shfl_xor(v, o);
  return v;
}
__device__ __forceinline__ float wave_max(float v) {
  for (int o = 32; o >= 1; o >>= 1) v = fmaxf(v, __shfl_xor(v, o));
  return v;
}

constexpr int D = 448, HQ = 16, HKV = 8, SK = 8192, DH = 256, F = 1792;

__device__ void ln_wave(const float* xs  , float* out  , int lane) {
  float s = 0.f;
  for (int i = 0; i < 7; ++i) s += xs[lane + 64 * i];
  s = wave_sum(s);
  float mean = s / 448.f;
  float v = 0.f;
  for (int i = 0; i < 7; ++i) { float d = xs[lane + 64 * i] - mean; v += d * d; }
  v = wave_sum(v) / 448.f;
  float mb = rbf(mean), vb = rbf(v);
  float den = rbf(sqrtf(rbf(vb + 1e-5f)));
  for (int i = 0; i < 7; ++i) out[lane + 64 * i] = rbf(rbf(xs[lane + 64 * i] - mb) / den);
}

__global__ void k_q(const u16* x, const u16* wq, float* q) {
  __shared__ float xs[448], xn[448], red[4][64];
  int t = threadIdx.x;
  for (int i = t; i < 448; i += 256) xs[i] = bf2f(x[i]);
  __syncthreads();
  if (t < 64) ln_wave(xs, xn, t);
  __syncthreads();
  int col = blockIdx.x * 64 + (t & 63), rg = t >> 6;
  float acc = 0.f;
  for (int r = rg; r < 448; r += 4) acc += xn[r] * bf2f(wq[(size_t)r * 4096 + col]);
  red[rg][t & 63] = acc;
  __syncthreads();
  if (t < 64) q[col] = rbf(red[0][t] + red[1][t] + red[2][t] + red[3][t]);
}

__global__ void k_attn(const u16* kc, const u16* vc, const float* q, float* part_o, float* part_ml) {
  __shared__ float qs[2][256], p[2][256], red[2][4];
  int s = blockIdx.x, g = blockIdx.y, t = threadIdx.x;
  qs[0][t] = q[(2 * g) * 256 + t];
  qs[1][t] = q[(2 * g + 1) * 256 + t];
  __syncthreads();
  const u16* kr = kc + ((size_t)g * SK + s * 256 + t) * DH;
  float a0 = 0.f, a1 = 0.f;
  for (int d = 0; d < 256; d += 8) {
    v4u w = *(const v4u*)(kr + d);
    for (int j = 0; j < 4; ++j) {
      float lo = bf2f(w[j] & 0xffff), hi = bf2f(w[j] >> 16);
      a0 += lo * qs[0][d + 2 * j] + hi * qs[0][d + 2 * j + 1];
      a1 += lo * qs[1][d + 2 * j] + hi * qs[1][d + 2 * j + 1];
    }
  }
  a0 *= 0.0625f; a1 *= 0.0625f;
  float m0 = wave_max(a0), m1 = wave_max(a1);
  if ((t & 63) == 0) { red[0][t >> 6] = m0; red[1][t >> 6] = m1; }
  __syncthreads();
  m0 = fmaxf(fmaxf(red[0][0], red[0][1]), fmaxf(red[0][2], red[0][3]));
  m1 = fmaxf(fmaxf(red[1][0], red[1][1]), fmaxf(red[1][2], red[1][3]));
  __syncthreads();
  float p0 = __expf(a0 - m0), p1 = __expf(a1 - m1);
  p[0][t] = p0; p[1][t] = p1;
  float l0 = wave_sum(p0), l1 = wave_sum(p1);
  if ((t & 63) == 0) { red[0][t >> 6] = l0; red[1][t >> 6] = l1; }
  __syncthreads();
  l0 = red[0][0] + red[0][1] + red[0][2] + red[0][3];
  l1 = red[1][0] + red[1][1] + red[1][2] + red[1][3];
  const u16* vr = vc + ((size_t)g * SK + s * 256) * DH + t;
  float o0 = 0.f, o1 = 0.f;
  for (int k = 0; k < 256; ++k) { float v = bf2f(vr[(size_t)k * DH]); o0 += p[0][k] * v; o1 += p[1][k] * v; }
  part_o[((2 * g) * 32 + s) * 256 + t] = o0;
  part_o[((2 * g + 1) * 32 + s) * 256 + t] = o1;
  if (t == 0) {
    part_ml[((2 * g) * 32 + s) * 2] = m0; part_ml[((2 * g) * 32 + s) * 2 + 1] = l0;
    part_ml[((2 * g + 1) * 32 + s) * 2] = m1; part_ml[((2 * g + 1) * 32 + s) * 2 + 1] = l1;
  }
}

__global__ void k_comb(const float* part_o, const float* part_ml, float* o) {
  int h = blockIdx.x, t = threadIdx.x;
  float M = -INFINITY;
  for (int s = 0; s < 32; ++s) M = fmaxf(M, part_ml[(h * 32 + s) * 2]);
  float L = 0.f, acc = 0.f;
  for (int s = 0; s < 32; ++s) {
    float w = __expf(part_ml[(h * 32 + s) * 2] - M);
    L += w * part_ml[(h * 32 + s) * 2 + 1];
    acc += w * part_o[(h * 32 + s) * 256 + t];
  }
  o[h * 256 + t] = rbf(acc / L);
}

__global__ void k_wo(const u16* x, const float* o, const u16* wo, float* h) {
  __shared__ float red[16][64];
  int t = threadIdx.x, col = blockIdx.x * 64 + (t & 63), rg = t >> 6;
  float acc = 0.f;
  for (int r = rg; r < 4096; r += 16) acc += o[r] * bf2f(wo[(size_t)r * 448 + col]);
  red[rg][t & 63] = acc;
  __syncthreads();
  if (t < 64) { float s = 0.f; for (int i = 0; i < 16; ++i) s += red[i][t]; h[col] = rbf(bf2f(x[col]) + rbf(s)); }
}

__device__ __forceinline__ float gelu_tanh(float x) {
  float u = 0.7978845608028654f * (x + 0.044715f * x * x * x);
  return 0.5f * x * (1.f + tanhf(u));
}

__global__ void k_gu(const float* h, const u16* wg, const u16* wu, float* a) {
  __shared__ float xs[448], xn[448], red[2][4][64];
  int t = threadIdx.x;
  for (int i = t; i < 448; i += 256) xs[i] = h[i];
  __syncthreads();
  if (t < 64) ln_wave(xs, xn, t);
  __syncthreads();
  int col = blockIdx.x * 64 + (t & 63), rg = t >> 6;
  float ag = 0.f, au = 0.f;
  for (int r = rg; r < 448; r += 4) { ag += xn[r] * bf2f(wg[(size_t)r * F + col]); au += xn[r] * bf2f(wu[(size_t)r * F + col]); }
  red[0][rg][t & 63] = ag; red[1][rg][t & 63] = au;
  __syncthreads();
  if (t < 64) {
    float g = rbf(red[0][0][t] + red[0][1][t] + red[0][2][t] + red[0][3][t]);
    float u = rbf(red[1][0][t] + red[1][1][t] + red[1][2][t] + red[1][3][t]);
    a[col] = rbf(rbf(gelu_tanh(g)) * u);
  }
}

__global__ void k_down(const float* h, const float* a, const u16* wd, u16* out) {
  __shared__ float red[16][64];
  int t = threadIdx.x, col = blockIdx.x * 64 + (t & 63), rg = t >> 6;
  float acc = 0.f;
  for (int r = rg; r < F; r += 16) acc += a[r] * bf2f(wd[(size_t)r * 448 + col]);
  red[rg][t & 63] = acc;
  __syncthreads();
  if (t < 64) { float s = 0.f; for (int i = 0; i < 16; ++i) s += red[i][t]; out[col] = (u16)f2bf(h[col] + rbf(s)); }
}

extern "C" void kernel_launch(void* const* d_in, const int* in_sizes, int n_in,
                              void* d_out, int out_size, void* d_ws, size_t ws_size,
                              hipStream_t stream) {
  const u16* x = (const u16*)d_in[0];
  const u16* kc = (const u16*)d_in[1];
  const u16* vc = (const u16*)d_in[2];
  const u16* wq = (const u16*)d_in[3];
  const u16* wo = (const u16*)d_in[4];
  const u16* wg = (const u16*)d_in[6];
  const u16* wu = (const u16*)d_in[7];
  const u16* wd = (const u16*)d_in[8];
  float* ws = (float*)d_ws;
  float* q = ws;
  float* part_o = ws + 4096;
  float* part_ml = part_o + 16 * 32 * 256;
  float* o = part_ml + 1024;
  float* h = o + 4096;
  float* a = h + 512;
  k_q<<<64, 256, 0, stream>>>(x, wq, q);
  k_attn<<<dim3(32, 8), 256, 0, stream>>>(kc, vc, q, part_o, part_ml);
  k_comb<<<16, 256, 0, stream>>>(part_o, part_ml, o);
  k_wo<<<7, 1024, 0, stream>>>(x, o, wo, h);
  k_gu<<<28, 256, 0, stream>>>(h, wg, wu, a);
  k_down<<<7, 1024, 0, stream>>>(h, a, wd, (u16*)d_out);
}
```

```cpp
#include <hip/hip_runtime.h>
#include <stdint.h>

typedef unsigned short u16;
typedef unsigned long long u64;
typedef unsigned v4u __attribute__((ext_vector_type(4)));
typedef unsigned v2u __attribute__((ext_vector_type(2)));
typedef float f32x4 __attribute__((ext_vector_type(4)));
typedef short bf16x8 __attribute__((ext_vector_type(8)));
#define LAS __attribute__((address_space(3)))

#define STAMPS 0

constexpr int AUX_SC1 = 16;
constexpr unsigned WS_E = 0;
constexpr unsigned WS_READY = 4096;
constexpr unsigned WS_QG = 8192;
constexpr unsigned WS_ML = 139264;
constexpr unsigned WS_Y = 143360;
constexpr unsigned WS_CS = 200704;
constexpr unsigned WS_G = 301056;
constexpr unsigned WS_PART = 501760;
constexpr unsigned WS_F = 1026048;
constexpr unsigned WS_END = 1427456;
constexpr unsigned WS_STAMP = 2u << 20;
constexpr unsigned SPIN_CAP = 1u << 17;
#define QDELAY 16
#define T2DELAY 8
#define T3DELAY 8
#define T3BDELAY 8
#define T4DELAY 40
#define PSLEEP 4

__device__ __forceinline__ float asf(unsigned u) { return __builtin_bit_cast(float, u); }
__device__ __forceinline__ unsigned asu(float f) { return __builtin_bit_cast(unsigned, f); }
__device__ __forceinline__ float bflo(unsigned w) { return asf(w << 16); }
__device__ __forceinline__ float bfhi(unsigned w) { return asf(w & 0xffff0000u); }
__device__ __forceinline__ unsigned f2bf(float f) { __bf16 b = (__bf16)f; return (unsigned)__builtin_bit_cast(u16, b); }
__device__ __forceinline__ float rbf(float f) { return asf(f2bf(f) << 16); }
__device__ __forceinline__ unsigned enc(float f) { unsigned u = asu(f); u = (u == 0x80000000u) ? 0u : u; return u ^ 0x80000000u; }
__device__ __forceinline__ float dec(unsigned u) { return asf(u ^ 0x80000000u); }

template <int CTRL> __device__ __forceinline__ float dpp(float x) {
  return asf((unsigned)__builtin_amdgcn_mov_dpp((int)asu(x), CTRL, 0xf, 0xf, true));
}
constexpr int XOR1 = 0xB1, XOR2 = 0x4E, ROR4 = 0x124, ROR8 = 0x128;
__device__ __forceinline__ float tr32(float a, float b) {
  auto r = __builtin_amdgcn_permlane32_swap(asu(a), asu(b), false, false);
  return asf(r[0]) + asf(r[1]);
}
__device__ __forceinline__ float tr16(float a, float b) {
  auto r = __builtin_amdgcn_permlane16_swap(asu(a), asu(b), false, false);
  return asf(r[0]) + asf(r[1]);
}
template <int CTRL> __device__ __forceinline__ float trd(float a, float b, bool bit) {
  const float t = bit ? a : b, k = bit ? b : a;
  return k + dpp<CTRL>(t);
}
__device__ __forceinline__ float mx32(float a) {
  auto r = __builtin_amdgcn_permlane32_swap(asu(a), asu(a), false, false);
  return fmaxf(asf(r[0]), asf(r[1]));
}
__device__ __forceinline__ float mx16(float a) {
  auto r = __builtin_amdgcn_permlane16_swap(asu(a), asu(a), false, false);
  return fmaxf(asf(r[0]), asf(r[1]));
}
__device__ __forceinline__ float wave_sum(float x) {
  x += dpp<XOR1>(x); x += dpp<XOR2>(x); x += dpp<ROR8>(x); x += dpp<ROR4>(x);
  x = tr16(x, x); x = tr32(x, x);
  return x;
}
__device__ __forceinline__ float wave_max(float x) {
  x = fmaxf(x, dpp<XOR1>(x)); x = fmaxf(x, dpp<XOR2>(x)); x = fmaxf(x, dpp<ROR8>(x)); x = fmaxf(x, dpp<ROR4>(x));
  x = mx16(x); x = mx32(x);
  return x;
}
__device__ __forceinline__ float red8_b345(float (&a)[8], bool b3) {
#pragma unroll
  for (int c = 0; c < 4; ++c) a[c] = tr32(a[c], a[c + 4]);
#pragma unroll
  for (int c = 0; c < 2; ++c) a[c] = tr16(a[c], a[c + 2]);
  return trd<ROR8>(a[0], a[1], b3);
}
#define KVPOL 0
__device__ __forceinline__ v4u ldnt(const u16* p) {
  return *(const v4u*)p;
}
__device__ __forceinline__ f32x4 mfma(v4u a, v4u b, f32x4 c) {
  return __builtin_amdgcn_mfma_f32_16x16x32_bf16(__builtin_bit_cast(bf16x8, a), __builtin_bit_cast(bf16x8, b), c, 0, 0, 0);
}
#define LGKM0() asm volatile("s_waitcnt lgkmcnt(0)" ::: "memory")
#define VM0() asm volatile("s_waitcnt vmcnt(0)" ::: "memory")
#define CBAR() asm volatile("" ::: "memory")
#define LDS_INC(f) __hip_atomic_fetch_add((LAS unsigned*)(lds + L_FLAG + (f)), 1u, __ATOMIC_RELAXED, __HIP_MEMORY_SCOPE_WORKGROUP)

constexpr int L_Q = 0;
constexpr int L_QP = 256;
constexpr int L_P = 704;
constexpr int L_MW = 1216;
constexpr int L_LW = 1232;
constexpr int L_WTS = 1248;
constexpr int L_FLAG = 1280;
constexpr int L_TAG = 1312;
constexpr int L_CST = 1328;
constexpr int L_RED2 = 1344;
constexpr int L_HP = 2368;
constexpr int L_RED3 = 2880;
constexpr int L_CSP = 3904;
constexpr int L_RED4 = 4928;
constexpr int L_H4 = 5152;
constexpr int L_RED = 5184;
constexpr int L_TOTAL = L_RED + 4096;
constexpr int F_QD = 0, F_R = 1, F_MC = 2, F_LC = 3, F_VC = 4, F_T = 5, F_QC = 6, F_EI = 7, F_P0 = 8, F_KI0 = 16,
              F_Y4 = 24, F_W2 = 25, F_QP = 26, F_CSC = 27, F_CS = 28;

__global__ void __launch_bounds__(1024) fused_decode(const u16* __restrict__ x, const u16* __restrict__ kc, const u16* __restrict__ vc,
                                                     const u16* __restrict__ wq, const u16* __restrict__ wo, const u16* __restrict__ wg,
                                                     const u16* __restrict__ wu, const u16* __restrict__ wd, u16* __restrict__ out,
                                                     unsigned char* __restrict__ ws) {
  __shared__ __attribute__((aligned(16))) unsigned lds_raw[L_TOTAL];
  LAS unsigned* lds = (LAS unsigned*)lds_raw;
  LAS float* ldf = (LAS float*)lds_raw;
  volatile LAS unsigned* flg = (volatile LAS unsigned*)(lds + L_FLAG);
  const int tid = threadIdx.x, lane = tid & 63, wave = __builtin_amdgcn_readfirstlane(tid >> 6);
  const int g = blockIdx.x & 7, j = blockIdx.x >> 3;
  const bool t2 = j < 28;
  const int jj = t2 ? j : 0;
  const int hq = 2 * g + jj / 14, rh = (jj % 14) / 7, cb7 = jj % 7;
  const bool t3a = (g < 7) && t2;
  const int cb = 4 * g + jj / 7, rb = jj % 7;
  const bool t3b = g < 7;
  const int fidx = 32 * g + j;
  const bool t4 = (g == 7) && t2;
  const __amdgpu_buffer_rsrc_t rs = __builtin_amdgcn_make_buffer_rsrc((void*)ws, 0, (int)WS_END, 0x00020000);
#define STAMP(i) do { } while (0)
  if (tid < 32) lds[L_FLAG + tid] = 0u;
  __syncthreads();

  const bool b0 = lane & 1, b1 = lane & 2, b3 = lane & 8;
  const int lb4 = (lane >> 4) & 1, lb5 = (lane >> 5) & 1;
  v4u wo_r, wgu_r;
  v2u wd_r[8];
  const u16* wo_p = wo + (size_t)(hq * 256 + 128 * rh + 8 * wave + 4 * (lane & 1) + 2 * lb5 + lb4) * 448 + 64 * cb7 + 8 * ((lane >> 1) & 7);
  const u16* wgu_p = (wave < 8 ? wg : wu) + (size_t)(64 * rb + 8 * (wave & 7) + (lane >> 3)) * 1792 + 64 * cb + 8 * (lane & 7);

  if (wave < 8) {
    if (wave == 0) STAMP(0);
    const u16* kb = kc + ((size_t)g * 8192 + 256 * j + 32 * wave + ((lane >> 2) & 3)) * 256 + 8 * ((lane & 3) + 4 * (lane >> 4));
    v4u K[16];
    while (flg[F_EI] < 8u) __builtin_amdgcn_s_sleep(1);
    CBAR();
#pragma unroll
    for (int i = 0; i < 16; ++i) { K[i] = ldnt(kb + (i >> 1) * 1024 + (i & 1) * 128); CBAR(); }
    if (t2) wo_r = ldnt(wo_p);
    if (t3a) wgu_r = ldnt(wgu_p);
    CBAR();
    if (lane == 0) flg[F_KI0 + wave] = 1u;
    {
      while (flg[F_QP] == 0u) __builtin_amdgcn_s_sleep(1);
      CBAR();
      const unsigned tl = lds[L_TAG] & 0xffu, th = (lds[L_TAG] >> 8) | (lds[L_TAG + 1] << 24);
      __builtin_amdgcn_s_setprio(3);
      v4u qa, qb;
      unsigned spins = 0;
      bool ok = false;
      const int off = (int)(WS_QG + (8 * g + wave) * 2048 + (lane >> 5) * 512 + (lane & 31) * 16);
      __builtin_amdgcn_s_sleep(QDELAY);
      for (;;) {
        CBAR();
        if (!ok) {
          qa = __builtin_amdgcn_raw_buffer_load_b128(rs, off, 0, AUX_SC1);
          qb = __builtin_amdgcn_raw_buffer_load_b128(rs, off + 1024, 0, AUX_SC1);
          ok = ((qa[0] & 0xffu) == tl) & (qa[1] == th) & ((qa[2] & 0xffu) == tl) & (qa[3] == th) &
               ((qb[0] & 0xffu) == tl) & (qb[1] == th) & ((qb[2] & 0xffu) == tl) & (qb[3] == th);
        }
        if (__all(ok) || ++spins > SPIN_CAP) break;
        __builtin_amdgcn_s_sleep(8);
      }
      const float c0 = asf(qa[0] & 0xffffff00u) + asf(qb[0] & 0xffffff00u), c1 = asf(qa[2] & 0xffffff00u) + asf(qb[2] & 0xffffff00u);
      const float s0 = tr32(c0, c0), s1 = tr32(c1, c1);
      if (lane < 32) lds[L_Q + 32 * wave + lane] = f2bf(s0) | (f2bf(s1) << 16);
      LGKM0();
      if (lane == 0) LDS_INC(F_QD);
      __builtin_amdgcn_s_setprio(0);
      if (wave == 0) STAMP(2);
    }
    while (flg[F_QD] < 8u) __builtin_amdgcn_s_sleep(1);
    CBAR();
    v4u qf[2];
    {
      const int head = (lane >> 2) & 1;
      const LAS v4u* qp = (const LAS v4u*)(lds + L_Q + head * 128 + 4 * ((lane & 3) + 4 * (lane >> 4)));
      qf[0] = qp[0]; qf[1] = qp[16];
    }
    const int mykg = (lane & 3) | (((lane >> 3) & 1) << 2);
    float s = 0.f;
#pragma unroll
    for (int kg = 0; kg < 8; ++kg) {
      f32x4 acc = {0.f, 0.f, 0.f, 0.f};
      acc = mfma(K[2 * kg], qf[0], acc);
      acc = mfma(K[2 * kg + 1], qf[1], acc);
      float v = b1 ? (b0 ? acc[3] : acc[2]) : (b0 ? acc[1] : acc[0]);
      v += dpp<XOR1>(v); v += dpp<XOR2>(v);
      s = (mykg == kg) ? v : s;
    }
    s = rbf(s) * 0.0625f;
    float m = s;
    m = fmaxf(m, dpp<XOR1>(m)); m = fmaxf(m, dpp<XOR2>(m)); m = fmaxf(m, dpp<ROR8>(m)); m = mx16(m); m = mx32(m);
    if ((lane & ~4) == 0) ldf[L_MW + wave * 2 + (lane >> 2)] = m;
    LGKM0();
    if (lane == 0) LDS_INC(F_MC);
    while (flg[F_MC] < 8u) __builtin_amdgcn_s_sleep(1);
    CBAR();
    {
      const int head = (lane >> 2) & 1;
      float mm = ldf[L_MW + head];
#pragma unroll
      for (int w = 1; w < 8; ++w) mm = fmaxf(mm, ldf[L_MW + 2 * w + head]);
      m = mm;
    }
    const float p = __builtin_amdgcn_exp2f((s - m) * 1.4426950408889634f);
    const unsigned pb = f2bf(p);
    lds[L_P + wave * 64 + ((lane >> 2) & 1) * 32 + (lane >> 4) * 8 + mykg] = pb | (pb << 16);
    float l = asf(pb << 16);
    l += dpp<XOR1>(l); l += dpp<XOR2>(l); l += dpp<ROR8>(l); l = tr16(l, l); l = tr32(l, l);
    if ((lane & ~4) == 0) ldf[L_LW + wave * 2 + (lane >> 2)] = l;
    LGKM0();
    unsigned old = 0;
    if (lane == 0) { flg[F_P0 + wave] = 1u; old = LDS_INC(F_LC); }
    old = __builtin_amdgcn_readfirstlane(old);
    if (old == 7u) {
      while (flg[F_T] == 0u) __builtin_amdgcn_s_sleep(1);
      CBAR();
      __builtin_amdgcn_s_setprio(3);
      if ((lane & ~4) == 0) {
        const int head = lane >> 2;
        float ls = 0.f;
#pragma unroll
        for (int w = 0; w < 8; ++w) ls += ldf[L_LW + 2 * w + head];
        v2u rec = {enc(m), enc(ls)};
        __builtin_amdgcn_raw_buffer_store_b64(rec, rs, (int)(WS_ML + ((2 * g + head) * 32 + j) * 8), 0, AUX_SC1);
      }
      __builtin_amdgcn_s_setprio(0);
    }
    if (wave == 0) STAMP(4);
    if (t3a) {
      float a[8];
  #pragma unroll
      for (int c = 0; c < 4; ++c) { a[2 * c] = bflo(wgu_r[c]); a[2 * c + 1] = bfhi(wgu_r[c]); }
      const float z = red8_b345(a, b3);
      ldf[L_CSP + wave * 64 + 8 * (lane & 7) + 4 * lb5 + 2 * lb4 + (lane >> 3 & 1)] = z;
      LGKM0();
      if (lane == 0) LDS_INC(F_CSC);
      if (wave == 4 || wave == 5) {
        while (flg[F_CSC] < 16u || flg[F_T] == 0u) __builtin_amdgcn_s_sleep(4);
        CBAR();
        float cs_ = 0.f;
  #pragma unroll
        for (int w = 0; w < 8; ++w) cs_ += ldf[L_CSP + ((wave - 4) * 8 + w) * 64 + lane];
        __builtin_amdgcn_raw_buffer_store_b32(enc(cs_), rs, (int)(WS_CS + (cb * 7 + rb) * 512 + (wave - 4) * 256 + lane * 4), 0, AUX_SC1);
      }
    }

    if (wave == 7) {
      while (flg[F_T] == 0u) __builtin_amdgcn_s_sleep(2);
      CBAR();
      __builtin_amdgcn_s_setprio(3);
      const unsigned t0 = lds[L_TAG], t1 = lds[L_TAG + 1];
      unsigned spins = 0;
      for (;;) {
        CBAR();
        const v4u r0 = __builtin_amdgcn_raw_buffer_load_b128(rs, (int)(WS_READY + lane * 16), 0, AUX_SC1);
        const v4u r1 = __builtin_amdgcn_raw_buffer_load_b128(rs, (int)(WS_READY + 1024 + lane * 16), 0, AUX_SC1);
        const bool ok = (r0[0] == t0) & (r0[1] == t1) & (r0[2] == t0) & (r0[3] == t1) & (r1[0] == t0) & (r1[1] == t1) & (r1[2] == t0) & (r1[3] == t1);
        if (__all(ok) || ++spins > SPIN_CAP) break;
        __builtin_amdgcn_s_sleep(8);
      }
      if (lane == 0) {
        flg[F_R] = 1u;
        if (blockIdx.x == 0) __builtin_amdgcn_raw_buffer_store_b64(v2u{t0, t1}, rs, (int)WS_E, 0, AUX_SC1);
      }
      if (t2) {
        v2u rec = {1u, 1u};
        bool ok = lane >= 32;
        spins = 0;
        for (;;) {
          CBAR();
          if (!ok) { rec = __builtin_amdgcn_raw_buffer_load_b64(rs, (int)(WS_ML + (hq * 32 + lane) * 8), 0, AUX_SC1); ok = (rec[0] != 0u) & (rec[1] != 0u); }
          if (__all(ok) || ++spins > SPIN_CAP) break;
          __builtin_amdgcn_s_sleep(8);
        }
        const float ms = lane < 32 ? dec(rec[0]) : -INFINITY, lsum = lane < 32 ? dec(rec[1]) : 0.f;
        const float M = wave_max(ms);
        const float e = lane < 32 ? __builtin_amdgcn_exp2f((ms - M) * 1.4426950408889634f) : 0.f;
        const float L = wave_sum(e * lsum);
        if (lane < 32) ldf[L_WTS + lane] = e / L;
        LGKM0();
        if (lane == 0) flg[F_W2] = 1u;
      }
      __builtin_amdgcn_s_setprio(0);
      STAMP(5);
    }
    if (wave == 6 && t3b) {
      while (flg[F_R] == 0u) __builtin_amdgcn_s_sleep(4);
      const int o0 = (int)(WS_CS + ((fidx >> 3) * 7 + (lane >> 4)) * 512 + (((lane >> 3) & 1) * 64 + 8 * (fidx & 7) + (lane & 7)) * 4);
      unsigned c0 = 0u, c1 = lane < 48 ? 0u : 0x80000000u;
      unsigned spins = 0;
      for (;;) {
        CBAR();
        if (c0 == 0u) c0 = __builtin_amdgcn_raw_buffer_load_b32(rs, o0, 0, AUX_SC1);
        if (c1 == 0u) c1 = __builtin_amdgcn_raw_buffer_load_b32(rs, o0 + 2048, 0, AUX_SC1);
        if (__all((c0 != 0u) & (c1 != 0u)) || ++spins > SPIN_CAP) break;
        __builtin_amdgcn_s_sleep(16);
      }
      float cs_ = dec(c0) + dec(c1);
      cs_ = tr16(cs_, cs_); cs_ = tr32(cs_, cs_);
      if (lane < 16) ldf[L_CST + lane] = cs_;
      LGKM0();
      if (lane == 0) flg[F_CS] = 1u;
    }
  } else {
    const int v = wave - 8;
    __builtin_amdgcn_s_setprio(3);
    const v2u ev = __builtin_amdgcn_raw_buffer_load_b64(rs, (int)WS_E, 0, AUX_SC1);
    if (v < 7) {
      const int r0 = 112 * (j & 3) + 16 * v + (lane >> 3);
      const v4u xv = *(const v4u*)(x + 8 * (lane < 56 ? lane : 55));
      const unsigned xr0 = (unsigned)x[r0], xr1 = (unsigned)x[r0 + 8];
      const u16* wqp = wq + (size_t)r0 * 4096 + 64 * (8 * g + (j >> 2)) + 8 * (lane & 7);
      const v4u W0 = ldnt(wqp), W1 = ldnt(wqp + (size_t)8 * 4096);
      CBAR();
      if (lane == 0) LDS_INC(F_EI);
      CBAR();
      float xf[8];
#pragma unroll
      for (int i = 0; i < 4; ++i) { xf[2 * i] = bflo(xv[i]); xf[2 * i + 1] = bfhi(xv[i]); }
      float sm = ((xf[0] + xf[1]) + (xf[2] + xf[3])) + ((xf[4] + xf[5]) + (xf[6] + xf[7]));
      sm = lane < 56 ? sm : 0.f;
      const float mean = wave_sum(sm) * (1.f / 448.f);
      float vs = 0.f;
#pragma unroll
      for (int i = 0; i < 8; ++i) { const float d = xf[i] - mean; vs += d * d; }
      vs = lane < 56 ? vs : 0.f;
      const float var = wave_sum(vs) * (1.f / 448.f);
      const float mb = rbf(mean), den = rbf(__builtin_sqrtf(rbf(rbf(var) + 1e-5f)));
      const float xn0 = rbf(rbf(bflo(xr0) - mb) / den), xn1 = rbf(rbf(bflo(xr1) - mb) / den);
      float acc[8];
#pragma unroll
      for (int c = 0; c < 4; ++c) {
        acc[2 * c] = fmaf(xn1, bflo(W1[c]), xn0 * bflo(W0[c]));
        acc[2 * c + 1] = fmaf(xn1, bfhi(W1[c]), xn0 * bfhi(W0[c]));
      }
      const float qv = red8_b345(acc, b3);
      ldf[L_QP + v * 64 + 8 * (lane & 7) + 4 * lb5 + 2 * lb4 + (lane >> 3 & 1)] = qv;
      LGKM0();
      unsigned oldq = 0;
      if (lane == 0) oldq = LDS_INC(F_QC);
      oldq = __builtin_amdgcn_readfirstlane(oldq);
      if (oldq == 6u) {
        CBAR();
        const u64 T = (((u64)ev[1] << 32) | ev[0]) + 1ull;
        float qs = 0.f;
#pragma unroll
        for (int w = 0; w < 7; ++w) qs += ldf[L_QP + w * 64 + lane];
        const unsigned qbits = (asu(qs) + 0x80u) & 0xffffff00u;
        v2u gr = {qbits | ((unsigned)T & 0xffu), (unsigned)(T >> 8)};
        __builtin_amdgcn_raw_buffer_store_b64(gr, rs, (int)(WS_QG + (8 * g + (j >> 2)) * 2048 + (j & 3) * 512 + lane * 8), 0, AUX_SC1);
        if (lane == 0) { lds[L_TAG] = (unsigned)T; lds[L_TAG + 1] = (unsigned)(T >> 32); LGKM0(); flg[F_QP] = 1u; }
        STAMP(1);
      }
    } else {
      const v4u z4 = {0u, 0u, 0u, 0u};
      __builtin_amdgcn_raw_buffer_store_b128(z4, rs, (int)(WS_PART + ((2 * g) * 32 + j) * 1024 + 16 * lane), 0, AUX_SC1);
      __builtin_amdgcn_raw_buffer_store_b128(z4, rs, (int)(WS_PART + ((2 * g + 1) * 32 + j) * 1024 + 16 * lane), 0, AUX_SC1);
      if (lane < 2) __builtin_amdgcn_raw_buffer_store_b64(v2u{0u, 0u}, rs, (int)(WS_ML + ((2 * g + lane) * 32 + j) * 8), 0, AUX_SC1);
      if (t2 && lane < 16) __builtin_amdgcn_raw_buffer_store_b128(z4, rs, (int)(WS_Y + (cb7 * 32 + hq * 2 + rh) * 256 + 16 * lane), 0, AUX_SC1);
      if (t3a) {
        if (lane < 32) __builtin_amdgcn_raw_buffer_store_b128(z4, rs, (int)(WS_CS + (cb * 7 + rb) * 512 + 16 * lane), 0, AUX_SC1);
        __builtin_amdgcn_raw_buffer_store_b128(z4, rs, (int)(WS_G + (cb * 7 + rb) * 1024 + 16 * lane), 0, AUX_SC1);
      }
      if (t3b) {
        __builtin_amdgcn_raw_buffer_store_b128(z4, rs, (int)(WS_F + fidx * 1792 + 16 * lane), 0, AUX_SC1);
        if (lane < 48) __builtin_amdgcn_raw_buffer_store_b128(z4, rs, (int)(WS_F + fidx * 1792 + 1024 + 16 * lane), 0, AUX_SC1);
      }
      CBAR();
      if (lane == 0) LDS_INC(F_EI);
      VM0();
      const u64 T = (((u64)ev[1] << 32) | ev[0]) + 1ull;
      if (lane == 0) {
        __builtin_amdgcn_raw_buffer_store_b64(v2u{(unsigned)T, (unsigned)(T >> 32)}, rs, (int)(WS_READY + blockIdx.x * 8), 0, AUX_SC1);
        flg[F_T] = 1u;
      }
      STAMP(3);
    }
    __builtin_amdgcn_s_setprio(0);
    const u16* vb = vc + ((size_t)g * 8192 + 256 * j + 32 * v + (lane >> 4)) * 256 + 8 * (lane & 15);
    v4u V[16];
    while (flg[F_KI0 + v] == 0u) __builtin_amdgcn_s_sleep(1);
    CBAR();
    if (t2) wo_r = ldnt(wo_p);
    if (t3a) wgu_r = ldnt(wgu_p);
    if (t3b && v < 2) {
      const int tc = (tid - 512) < 112 ? (tid - 512) : 111;
#pragma unroll
      for (int r = 0; r < 8; ++r) wd_r[r] = __builtin_nontemporal_load((const v2u*)(wd + (size_t)(8 * fidx + r) * 448 + 4 * tc));
    }
    CBAR();
#pragma unroll
    for (int i = 0; i < 16; ++i) { V[i] = ldnt(vb + (i >> 1) * 1024 + (i & 1) * 128); CBAR(); }
    __builtin_amdgcn_s_setprio(2);
    if (t3a) {
      float a[8];
#pragma unroll
      for (int c = 0; c < 4; ++c) { a[2 * c] = bflo(wgu_r[c]); a[2 * c + 1] = bfhi(wgu_r[c]); }
      const float z = red8_b345(a, b3);
      ldf[L_CSP + wave * 64 + 8 * (lane & 7) + 4 * lb5 + 2 * lb4 + (lane >> 3 & 1)] = z;
      LGKM0();
      if (lane == 0) LDS_INC(F_CSC);
    }
    while (flg[F_P0 + v] == 0u) __builtin_amdgcn_s_sleep(2);
    CBAR();
    const LAS v4u* pp = (const LAS v4u*)(lds + L_P + v * 64 + ((lane >> 3) & 1) * 32 + (lane >> 4) * 8);
    const v4u pa = pp[0], pb = pp[1];
    const unsigned hm = (lane & 1) ? 0xffff0000u : 0x0000ffffu;
    const int rsel = (lane >> 1) & 3;
    const unsigned m0 = rsel == 0 ? hm : 0u, m1 = rsel == 1 ? hm : 0u, m2 = rsel == 2 ? hm : 0u, m3 = rsel == 3 ? hm : 0u;
    f32x4 a0 = {0.f, 0.f, 0.f, 0.f}, a1 = {0.f, 0.f, 0.f, 0.f};
#pragma unroll
    for (int kg = 0; kg < 8; ++kg) {
      const unsigned pd = kg < 4 ? pa[kg & 3] : pb[kg & 3];
      const v4u A = {pd & m0, pd & m1, pd & m2, pd & m3};
      a0 = mfma(A, V[2 * kg], a0);
      a1 = mfma(A, V[2 * kg + 1], a1);
    }
    LAS f32x4* rp = (LAS f32x4*)(ldf + L_RED + v * 512 + (lane >> 5) * 256 + 8 * (lane & 15) + 4 * ((lane >> 4) & 1));
    rp[0] = a0; rp[32] = a1;
    LGKM0();
    unsigned oldv = 0;
    if (lane == 0) oldv = LDS_INC(F_VC);
    oldv = __builtin_amdgcn_readfirstlane(oldv);
    if (oldv == 7u) {
      CBAR();
      STAMP(6);
      const LAS f32x4* sp = (const LAS f32x4*)(ldf + L_RED + 4 * lane);
      f32x4 r0[8], r1[8];
#pragma unroll
      for (int w = 0; w < 8; ++w) { r0[w] = sp[w * 128]; r1[w] = sp[w * 128 + 64]; }
      const f32x4 s0 = ((r0[0] + r0[1]) + (r0[2] + r0[3])) + ((r0[4] + r0[5]) + (r0[6] + r0[7]));
      const f32x4 s1 = ((r1[0] + r1[1]) + (r1[2] + r1[3])) + ((r1[4] + r1[5]) + (r1[6] + r1[7]));
      v4u e0 = {enc(s0[0]), enc(s0[1]), enc(s0[2]), enc(s0[3])};
      v4u e1 = {enc(s1[0]), enc(s1[1]), enc(s1[2]), enc(s1[3])};
      __builtin_amdgcn_raw_buffer_store_b128(e0, rs, (int)(WS_PART + ((2 * g) * 32 + j) * 1024 + 16 * lane), 0, AUX_SC1);
      __builtin_amdgcn_raw_buffer_store_b128(e1, rs, (int)(WS_PART + ((2 * g + 1) * 32 + j) * 1024 + 16 * lane), 0, AUX_SC1);
      STAMP(7);
    }
  }
  if (t2) {
    {
      while (flg[F_VC] < 8u || flg[F_R] == 0u) __builtin_amdgcn_s_sleep(1);
      CBAR();
      v4u pa;
      unsigned spins = 0;
      const int off = (int)(WS_PART + (hq * 32 + (lane >> 1)) * 1024 + (128 * rh + 8 * wave + 4 * (lane & 1)) * 4);
      bool ok = false;
      __builtin_amdgcn_s_sleep(T2DELAY);
      for (;;) {
        CBAR();
        if (!ok) {
          pa = __builtin_amdgcn_raw_buffer_load_b128(rs, off, 0, AUX_SC1);
          ok = (pa[0] != 0u) & (pa[1] != 0u) & (pa[2] != 0u) & (pa[3] != 0u);
        }
        if (__all(ok) || ++spins > SPIN_CAP) break;
        __builtin_amdgcn_s_sleep(PSLEEP);
      }
      if (wave == 0) STAMP(8);
      if (wave == 15) STAMP(21);
      if (wave == 7) STAMP(22);
      while (flg[F_W2] == 0u) __builtin_amdgcn_s_sleep(1);
      CBAR();
      const float w = ldf[L_WTS + (lane >> 1)];
      const float u0 = tr32(w * dec(pa[0]), w * dec(pa[2])), u1 = tr32(w * dec(pa[1]), w * dec(pa[3]));
      float o = tr16(u0, u1);
      o += dpp<XOR2>(o); o += dpp<ROR8>(o); o += dpp<ROR4>(o);
      o = rbf(o);
      float y[8];
#pragma unroll
      for (int c = 0; c < 4; ++c) { y[2 * c] = o * bflo(wo_r[c]); y[2 * c + 1] = o * bfhi(wo_r[c]); }
#pragma unroll
      for (int c = 0; c < 4; ++c) y[c] = tr32(y[c], y[c + 4]);
#pragma unroll
      for (int c = 0; c < 2; ++c) y[c] = tr16(y[c], y[c + 2]);
      const float yy = trd<XOR1>(y[0], y[1], b0);
      ldf[L_RED2 + wave * 64 + 8 * ((lane >> 1) & 7) + 4 * lb5 + 2 * lb4 + (lane & 1)] = yy;
    }
    __syncthreads();
    if (wave == 0) {
      STAMP(18);
      const LAS f32x4* rp = (const LAS f32x4*)(ldf + L_RED2 + (lane >> 4) * 256 + 4 * (lane & 15));
      const f32x4 a = (rp[0] + rp[16]) + (rp[32] + rp[48]);
      const float u0 = tr32(a[0], a[2]), u1 = tr32(a[1], a[3]);
      const float z = tr16(u0, u1);
      __builtin_amdgcn_raw_buffer_store_b32(enc(z), rs, (int)(WS_Y + (cb7 * 32 + hq * 2 + rh) * 256 + (4 * (lane & 15) + 2 * lb5 + lb4) * 4), 0, AUX_SC1);
      STAMP(9);
    }
  }
  if (t3a) {
    const unsigned xraw = (unsigned)x[64 * rb + 8 * (wave & 7) + (lane >> 3)];
    if (wave >= 8) {
      v4u yv;
      unsigned spins = 0;
      bool ok = false;
      const int off = (int)(WS_Y + (rb * 32 + 4 * (wave - 8) + (lane >> 4)) * 256 + (lane & 15) * 16);
      __builtin_amdgcn_s_sleep(T3DELAY);
      for (;;) {
        CBAR();
        if (!ok) {
          yv = __builtin_amdgcn_raw_buffer_load_b128(rs, off, 0, AUX_SC1);
          ok = (yv[0] != 0u) & (yv[1] != 0u) & (yv[2] != 0u) & (yv[3] != 0u);
        }
        if (__all(ok) || ++spins > SPIN_CAP) break;
        __builtin_amdgcn_s_sleep(PSLEEP);
      }
      if (wave == 8) STAMP(10);
      if (wave == 15) STAMP(19);
      if (wave == 12) STAMP(20);
      const float u0 = tr32(dec(yv[0]), dec(yv[2])), u1 = tr32(dec(yv[1]), dec(yv[3]));
      ldf[L_HP + (wave - 8) * 64 + 4 * (lane & 15) + 2 * lb5 + lb4] = tr16(u0, u1);
    }
    __syncthreads();
    if (wave == 0) STAMP(14);
    {
      const int row = 8 * (wave & 7) + (lane >> 3);
      float t8[8];
#pragma unroll
      for (int w = 0; w < 8; ++w) t8[w] = ldf[L_HP + w * 64 + row];
      const float at = ((t8[0] + t8[1]) + (t8[2] + t8[3])) + ((t8[4] + t8[5]) + (t8[6] + t8[7]));
      const float h = rbf(bflo(xraw) + rbf(at));
      float a[8];
#pragma unroll
      for (int c = 0; c < 4; ++c) { a[2 * c] = h * bflo(wgu_r[c]); a[2 * c + 1] = h * bfhi(wgu_r[c]); }
      const float z = red8_b345(a, b3);
      ldf[L_RED3 + wave * 64 + 8 * (lane & 7) + 4 * lb5 + 2 * lb4 + (lane >> 3 & 1)] = z;
      if (wave < 8 && (lane & 7) == 0) ldf[L_QP + row] = h;
    }
    __syncthreads();
    if (wave == 0) STAMP(15);
    if (wave < 2) {
      float t8[8];
#pragma unroll
      for (int w = 0; w < 8; ++w) t8[w] = ldf[L_RED3 + (wave * 8 + w) * 64 + lane];
      const float z = ((t8[0] + t8[1]) + (t8[2] + t8[3])) + ((t8[4] + t8[5]) + (t8[6] + t8[7]));
      __builtin_amdgcn_raw_buffer_store_b32(enc(z), rs, (int)(WS_G + (cb * 7 + rb) * 1024 + (lane >> 3) * 128 + (8 * wave + (lane & 7)) * 4), 0, AUX_SC1);
    } else if (wave == 2) {
      const float h = ldf[L_QP + lane];
      const float piv = asf((unsigned)__builtin_amdgcn_readfirstlane((int)asu(h)));
      const float d = h - piv;
      float st = tr32(d, d * d);
      st = tr16(st, st); st += dpp<XOR1>(st); st += dpp<XOR2>(st); st += dpp<ROR8>(st); st += dpp<ROR4>(st);
      const int k = lane >> 3;
      if (k == 0 || k == 1 || k == 4) {
        const float val = k == 0 ? piv : st;
        __builtin_amdgcn_raw_buffer_store_b32(enc(val), rs, (int)(WS_G + (cb * 7 + rb) * 1024 + (lane & 7) * 128 + (16 + (k == 4 ? 2 : k)) * 4), 0, AUX_SC1);
      }
    }
    if (wave == 0) STAMP(16);
  }
  if (t3b) {
    if (wave == 8 || wave == 9) {
      if (!t3a) { while (flg[F_VC] < 8u || flg[F_R] == 0u) __builtin_amdgcn_s_sleep(1); __builtin_amdgcn_s_sleep(40); }
      v4u gv = {1u, 1u, 1u, 1u};
      bool ok = (lane >= 56) || ((lane & 7) > 4);
      unsigned spins = 0;
      const int off = (int)(WS_G + ((fidx >> 3) * 7 + (lane >> 3)) * 1024 + (fidx & 7) * 128 + (lane & 7) * 16);
      __builtin_amdgcn_s_sleep(T3BDELAY);
      for (;;) {
        CBAR();
        if (!ok) {
          gv = __builtin_amdgcn_raw_buffer_load_b128(rs, off, 0, AUX_SC1);
          ok = (gv[0] != 0u) & (gv[1] != 0u) & (gv[2] != 0u) & ((gv[3] != 0u) | ((lane & 7) == 4));
        }
        if (__all(ok) || ++spins > SPIN_CAP) break;
        __builtin_amdgcn_s_sleep(PSLEEP);
      }
      if (wave == 8) STAMP(17);
      while (flg[F_CS] == 0u) __builtin_amdgcn_s_sleep(1);
      CBAR();
      const bool live = lane < 56;
      const bool isst = (lane & 7) == 4;
      const float piv = dec(gv[0]), s1 = dec(gv[1]), s2 = dec(gv[2]);
      const float dm = s1 * (1.f / 64.f);
      const float mw = piv + dm, m2 = s2 - s1 * dm;
      float a0 = live ? (isst ? mw : dec(gv[0])) : 0.f, a1 = live ? dec(gv[1]) : 0.f, a2 = live ? dec(gv[2]) : 0.f, a3 = live ? dec(gv[3]) : 0.f;
      a0 = tr32(a0, a0); a1 = tr32(a1, a1); a2 = tr32(a2, a2); a3 = tr32(a3, a3);
      a0 = tr16(a0, a0); a1 = tr16(a1, a1); a2 = tr16(a2, a2); a3 = tr16(a3, a3);
      a0 += dpp<ROR8>(a0); a1 += dpp<ROR8>(a1); a2 += dpp<ROR8>(a2); a3 += dpp<ROR8>(a3);
      const float mean = asf((unsigned)__builtin_amdgcn_readlane((int)asu(a0), 4)) * (1.f / 7.f);
      const float dd = mw - mean;
      float vv = (live && isst) ? m2 + 64.f * dd * dd : 0.f;
      vv = tr32(vv, vv); vv = tr16(vv, vv); vv += dpp<ROR8>(vv);
      const float var = fmaxf(asf((unsigned)__builtin_amdgcn_readlane((int)asu(vv), 4)) * (1.f / 448.f), 0.f);
      const float mb = rbf(mean), den = rbf(__builtin_sqrtf(rbf(rbf(var) + 1e-5f)));
      const float rden = 1.f / den;
      const f32x4 cs4 = *(const LAS f32x4*)(ldf + L_CST + 4 * (lane & 3));
      const float z0 = rbf((a0 - mb * cs4[0]) * rden), z1 = rbf((a1 - mb * cs4[1]) * rden), z2 = rbf((a2 - mb * cs4[2]) * rden), z3 = rbf((a3 - mb * cs4[3]) * rden);
      const float u0 = dpp<XOR2>(z0), u1 = dpp<XOR2>(z1), u2 = dpp<XOR2>(z2), u3 = dpp<XOR2>(z3);
      float av[4];
      {
        const float zz[4] = {z0, z1, z2, z3}, uu[4] = {u0, u1, u2, u3};
#pragma unroll
        for (int i = 0; i < 4; ++i) {
          const float z = zz[i];
          const float t = 0.7978845608028654f * (z + 0.044715f * z * z * z);
          const float e2 = __builtin_amdgcn_exp2f(t * 2.8853900817779268f);
          const float th = 1.f - 2.f * __builtin_amdgcn_rcpf(e2 + 1.f);
          av[i] = rbf(rbf(0.5f * z * (1.f + th)) * uu[i]);
        }
      }
      f32x4 f = {0.f, 0.f, 0.f, 0.f};
#pragma unroll
      for (int r = 0; r < 8; ++r) {
        const float ar = asf((unsigned)__builtin_amdgcn_readlane((int)asu(av[r & 3]), r >> 2));
        f[0] = fmaf(ar, bflo(wd_r[r][0]), f[0]); f[1] = fmaf(ar, bfhi(wd_r[r][0]), f[1]);
        f[2] = fmaf(ar, bflo(wd_r[r][1]), f[2]); f[3] = fmaf(ar, bfhi(wd_r[r][1]), f[3]);
      }
      if (tid - 512 < 112) {
        v4u e = {enc(f[0]), enc(f[1]), enc(f[2]), enc(f[3])};
        __builtin_amdgcn_raw_buffer_store_b128(e, rs, (int)(WS_F + fidx * 1792 + 16 * (tid - 512)), 0, AUX_SC1);
      }
      if (wave == 8) STAMP(11);
    }
  } else if (t4) {
    const int rid = j;
    {
      const int off = wave < 14 ? (int)(WS_F + (16 * wave + (lane >> 2)) * 1792 + (16 * rid + 4 * (lane & 3)) * 4)
                                : (int)(WS_Y + ((rid >> 2) * 32 + 16 * (wave - 14) + (lane >> 2)) * 256 + (16 * (rid & 3) + 4 * (lane & 3)) * 4);
      v4u pv;
      unsigned spins = 0;
      bool ok = false;
      if (wave < 14) { while (flg[F_Y4] < 2u) __builtin_amdgcn_s_sleep(1); __builtin_amdgcn_s_sleep(T4DELAY); }
      else __builtin_amdgcn_s_sleep(T3DELAY);
      for (;;) {
        CBAR();
        if (!ok) {
          pv = __builtin_amdgcn_raw_buffer_load_b128(rs, off, 0, AUX_SC1);
          ok = (pv[0] != 0u) & (pv[1] != 0u) & (pv[2] != 0u) & (pv[3] != 0u);
        }
        if (__all(ok) || ++spins > SPIN_CAP) break;
        __builtin_amdgcn_s_sleep(PSLEEP);
      }
      if (wave >= 14 && lane == 0) LDS_INC(F_Y4);
      if (wave == 0) STAMP(12);
      const float a0 = tr32(dec(pv[0]), dec(pv[2])), a1 = tr32(dec(pv[1]), dec(pv[3]));
      float z = tr16(a0, a1);
      z += dpp<ROR8>(z); z += dpp<ROR4>(z);
      const int col = 4 * (lane & 3) + 2 * lb5 + lb4;
      if ((lane & 12) == 0) ldf[(wave < 14 ? L_RED4 + wave * 16 : L_H4 + (wave - 14) * 16) + col] = z;
    }
    const unsigned xraw = (unsigned)x[16 * rid + (tid & 15)];
    __syncthreads();
    if (tid < 16) {
      float t[14];
#pragma unroll
      for (int w = 0; w < 14; ++w) t[w] = ldf[L_RED4 + w * 16 + tid];
      const float f = (((t[0] + t[1]) + (t[2] + t[3])) + ((t[4] + t[5]) + (t[6] + t[7]))) + (((t[8] + t[9]) + (t[10] + t[11])) + (t[12] + t[13]));
      const float h = rbf(bflo(xraw) + rbf(ldf[L_H4 + tid] + ldf[L_H4 + 16 + tid]));
      out[16 * rid + tid] = (u16)f2bf(h + rbf(f));
    }
    if (wave == 0) STAMP(13);
  }
}

extern "C" void kernel_launch(void* const* d_in, const int* in_sizes, int n_in,
                              void* d_out, int out_size, void* d_ws, size_t ws_size,
                              hipStream_t stream) {
  fused_decode<<<256, 1024, 0, stream>>>((const u16*)d_in[0], (const u16*)d_in[1], (const u16*)d_in[2], (const u16*)d_in[3],
                                         (const u16*)d_in[4], (const u16*)d_in[6], (const u16*)d_in[7], (const u16*)d_in[8],
                                         (u16*)d_out, (unsigned char*)d_ws);
}
```

```cpp
#include <hip/hip_runtime.h>
#include <stdint.h>

typedef unsigned short u16;
typedef unsigned long long u64;
typedef unsigned v4u __attribute__((ext_vector_type(4)));
typedef unsigned v2u __attribute__((ext_vector_type(2)));
typedef float f32x4 __attribute__((ext_vector_type(4)));
typedef short bf16x8 __attribute__((ext_vector_type(8)));
#define LAS __attribute__((address_space(3)))

#define STAMPS 0

constexpr int AUX_SC1 = 16;
constexpr unsigned WS_E = 0;
constexpr unsigned WS_READY = 4096;
constexpr unsigned WS_QG = 8192;
constexpr unsigned WS_ML = 139264;
constexpr unsigned WS_Y = 143360;
constexpr unsigned WS_CS = 200704;
constexpr unsigned WS_G = 301056;
constexpr unsigned WS_PART = 501760;
constexpr unsigned WS_F = 1026048;
constexpr unsigned WS_END = 1427456;
constexpr unsigned WS_STAMP = 2u << 20;
constexpr unsigned SPIN_CAP = 1u << 17;
#define CODEPF 1
#define CODEBYTES 13824
#define QDELAY 16
#define T2DELAY 8
#define T3DELAY 8
#define T3BDELAY 8
#define T4DELAY 40
#define PSLEEP 4

__device__ __forceinline__ float asf(unsigned u) { return __builtin_bit_cast(float, u); }
__device__ __forceinline__ unsigned asu(float f) { return __builtin_bit_cast(unsigned, f); }
__device__ __forceinline__ float bflo(unsigned w) { return asf(w << 16); }
__device__ __forceinline__ float bfhi(unsigned w) { return asf(w & 0xffff0000u); }
__device__ __forceinline__ unsigned f2bf(float f) { __bf16 b = (__bf16)f; return (unsigned)__builtin_bit_cast(u16, b); }
__device__ __forceinline__ float rbf(float f) { return asf(f2bf(f) << 16); }
__device__ __forceinline__ unsigned enc(float f) { unsigned u = asu(f); u = (u == 0x80000000u) ? 0u : u; return u ^ 0x80000000u; }
__device__ __forceinline__ float dec(unsigned u) { return asf(u ^ 0x80000000u); }

template <int CTRL> __device__ __forceinline__ float dpp(float x) {
  return asf((unsigned)__builtin_amdgcn_mov_dpp((int)asu(x), CTRL, 0xf, 0xf, true));
}
constexpr int XOR1 = 0xB1, XOR2 = 0x4E, ROR4 = 0x124, ROR8 = 0x128;
__device__ __forceinline__ float tr32(float a, float b) {
  auto r = __builtin_amdgcn_permlane32_swap(asu(a), asu(b), false, false);
  return asf(r[0]) + asf(r[1]);
}
__device__ __forceinline__ float tr16(float a, float b) {
  auto r = __builtin_amdgcn_permlane16_swap(asu(a), asu(b), false, false);
  return asf(r[0]) + asf(r[1]);
}
template <int CTRL> __device__ __forceinline__ float trd(float a, float b, bool bit) {
  const float t = bit ? a : b, k = bit ? b : a;
  return k + dpp<CTRL>(t);
}
__device__ __forceinline__ float mx32(float a) {
  auto r = __builtin_amdgcn_permlane32_swap(asu(a), asu(a), false, false);
  return fmaxf(asf(r[0]), asf(r[1]));
}
__device__ __forceinline__ float mx16(float a) {
  auto r = __builtin_amdgcn_permlane16_swap(asu(a), asu(a), false, false);
  return fmaxf(asf(r[0]), asf(r[1]));
}
__device__ __forceinline__ float wave_sum(float x) {
  x += dpp<XOR1>(x); x += dpp<XOR2>(x); x += dpp<ROR8>(x); x += dpp<ROR4>(x);
  x = tr16(x, x); x = tr32(x, x);
  return x;
}
__device__ __forceinline__ float wave_max(float x) {
  x = fmaxf(x, dpp<XOR1>(x)); x = fmaxf(x, dpp<XOR2>(x)); x = fmaxf(x, dpp<ROR8>(x)); x = fmaxf(x, dpp<ROR4>(x));
  x = mx16(x); x = mx32(x);
  return x;
}
__device__ __forceinline__ float red8_b345(float (&a)[8], bool b3) {
#pragma unroll
  for (int c = 0; c < 4; ++c) a[c] = tr32(a[c], a[c + 4]);
#pragma unroll
  for (int c = 0; c < 2; ++c) a[c] = tr16(a[c], a[c + 2]);
  return trd<ROR8>(a[0], a[1], b3);
}
#define KVPOL 1
__device__ __forceinline__ v4u ldnt(const u16* p) {
  return __builtin_nontemporal_load((const v4u*)p);
}
__device__ __forceinline__ f32x4 mfma(v4u a, v4u b, f32x4 c) {
  return __builtin_amdgcn_mfma_f32_16x16x32_bf16(__builtin_bit_cast(bf16x8, a), __builtin_bit_cast(bf16x8, b), c, 0, 0, 0);
}
#define LGKM0() asm volatile("s_waitcnt lgkmcnt(0)" ::: "memory")
#define VM0() asm volatile("s_waitcnt vmcnt(0)" ::: "memory")
#define CBAR() asm volatile("" ::: "memory")
#define LDS_INC(f) __hip_atomic_fetch_add((LAS unsigned*)(lds + L_FLAG + (f)), 1u, __ATOMIC_RELAXED, __HIP_MEMORY_SCOPE_WORKGROUP)

constexpr int L_Q = 0;
constexpr int L_QP = 256;
constexpr int L_P = 704;
constexpr int L_MW = 1216;
constexpr int L_LW = 1232;
constexpr int L_WTS = 1248;
constexpr int L_FLAG = 1280;
constexpr int L_TAG = 1312;
constexpr int L_CST = 1328;
constexpr int L_RED2 = 1344;
constexpr int L_HP = 2368;
constexpr int L_RED3 = 2880;
constexpr int L_CSP = 3904;
constexpr int L_RED4 = 4928;
constexpr int L_H4 = 5152;
constexpr int L_RED = 5184;
constexpr int L_TOTAL = L_RED + 4096;
constexpr int F_QD = 0, F_R = 1, F_MC = 2, F_LC = 3, F_VC = 4, F_T = 5, F_QC = 6, F_EI = 7, F_P0 = 8, F_KI0 = 16,
              F_Y4 = 24, F_W2 = 25, F_QP = 26, F_CSC = 27, F_CS = 28;

__global__ void __launch_bounds__(1024) fused_decode(const u16* __restrict__ x, const u16* __restrict__ kc, const u16* __restrict__ vc,
                                                     const u16* __restrict__ wq, const u16* __restrict__ wo, const u16* __restrict__ wg,
                                                     const u16* __restrict__ wu, const u16* __restrict__ wd, u16* __restrict__ out,
                                                     unsigned char* __restrict__ ws) {
  __shared__ __attribute__((aligned(16))) unsigned lds_raw[L_TOTAL];
  LAS unsigned* lds = (LAS unsigned*)lds_raw;
  LAS float* ldf = (LAS float*)lds_raw;
  volatile LAS unsigned* flg = (volatile LAS unsigned*)(lds + L_FLAG);
  const int tid = threadIdx.x, lane = tid & 63, wave = __builtin_amdgcn_readfirstlane(tid >> 6);
  const int g = blockIdx.x & 7, j = blockIdx.x >> 3;
  const bool t2 = j < 28;
  const int jj = t2 ? j : 0;
  const int hq = 2 * g + jj / 14, rh = (jj % 14) / 7, cb7 = jj % 7;
  const bool t3a = (g < 7) && t2;
  const int cb = 4 * g + jj / 7, rb = jj % 7;
  const bool t3b = g < 7;
  const int fidx = 32 * g + j;
  const bool t4 = (g == 7) && t2;
  const __amdgpu_buffer_rsrc_t rs = __builtin_amdgcn_make_buffer_rsrc((void*)ws, 0, (int)WS_END, 0x00020000);
#define STAMP(i) do { } while (0)
  unsigned cpf = 0u;
  if (wave < 2) {
    const u64 pc = (u64)__builtin_amdgcn_s_getpc();
    const int o = (wave * 64 + lane) * 128;
    if (o < CODEBYTES) cpf = *(const __attribute__((address_space(1))) unsigned*)(pc + o);
  }
  if (tid < 32) lds[L_FLAG + tid] = 0u;
  __syncthreads();

  const bool b0 = lane & 1, b1 = lane & 2, b3 = lane & 8;
  const int lb4 = (lane >> 4) & 1, lb5 = (lane >> 5) & 1;
  v4u wo_r, wgu_r;
  v2u wd_r[8];
  const u16* wo_p = wo + (size_t)(hq * 256 + 128 * rh + 8 * wave + 4 * (lane & 1) + 2 * lb5 + lb4) * 448 + 64 * cb7 + 8 * ((lane >> 1) & 7);
  const u16* wgu_p = (wave < 8 ? wg : wu) + (size_t)(64 * rb + 8 * (wave & 7) + (lane >> 3)) * 1792 + 64 * cb + 8 * (lane & 7);

  if (wave < 8) {
    if (wave == 0) STAMP(0);
    const u16* kb = kc + ((size_t)g * 8192 + 256 * j + 32 * wave + ((lane >> 2) & 3)) * 256 + 8 * ((lane & 3) + 4 * (lane >> 4));
    v4u K[16];
    while (flg[F_EI] < 8u) __builtin_amdgcn_s_sleep(1);
    CBAR();
#pragma unroll
    for (int i = 0; i < 16; ++i) { K[i] = ldnt(kb + (i >> 1) * 1024 + (i & 1) * 128); CBAR(); }
    if (t2) wo_r = ldnt(wo_p);
    if (t3a) wgu_r = ldnt(wgu_p);
    CBAR();
    if (lane == 0) flg[F_KI0 + wave] = 1u;
    {
      while (flg[F_QP] == 0u) __builtin_amdgcn_s_sleep(1);
      CBAR();
      const unsigned tl = lds[L_TAG] & 0xffu, th = (lds[L_TAG] >> 8) | (lds[L_TAG + 1] << 24);
      __builtin_amdgcn_s_setprio(3);
      v4u qa, qb;
      unsigned spins = 0;
      bool ok = false;
      const int off = (int)(WS_QG + (8 * g + wave) * 2048 + (lane >> 5) * 512 + (lane & 31) * 16);
      __builtin_amdgcn_s_sleep(QDELAY);
      for (;;) {
        CBAR();
        if (!ok) {
          qa = __builtin_amdgcn_raw_buffer_load_b128(rs, off, 0, AUX_SC1);
          qb = __builtin_amdgcn_raw_buffer_load_b128(rs, off + 1024, 0, AUX_SC1);
          ok = ((qa[0] & 0xffu) == tl) & (qa[1] == th) & ((qa[2] & 0xffu) == tl) & (qa[3] == th) &
               ((qb[0] & 0xffu) == tl) & (qb[1] == th) & ((qb[2] & 0xffu) == tl) & (qb[3] == th);
        }
        if (__all(ok) || ++spins > SPIN_CAP) break;
        __builtin_amdgcn_s_sleep(8);
      }
      const float c0 = asf(qa[0] & 0xffffff00u) + asf(qb[0] & 0xffffff00u), c1 = asf(qa[2] & 0xffffff00u) + asf(qb[2] & 0xffffff00u);
      const float s0 = tr32(c0, c0), s1 = tr32(c1, c1);
      if (lane < 32) lds[L_Q + 32 * wave + lane] = f2bf(s0) | (f2bf(s1) << 16);
      LGKM0();
      if (lane == 0) LDS_INC(F_QD);
      __builtin_amdgcn_s_setprio(0);
      if (wave == 0) STAMP(2);
    }
    while (flg[F_QD] < 8u) __builtin_amdgcn_s_sleep(1);
    CBAR();
    v4u qf[2];
    {
      const int head = (lane >> 2) & 1;
      const LAS v4u* qp = (const LAS v4u*)(lds + L_Q + head * 128 + 4 * ((lane & 3) + 4 * (lane >> 4)));
      qf[0] = qp[0]; qf[1] = qp[16];
    }
    const int mykg = (lane & 3) | (((lane >> 3) & 1) << 2);
    float s = 0.f;
#pragma unroll
    for (int kg = 0; kg < 8; ++kg) {
      f32x4 acc = {0.f, 0.f, 0.f, 0.f};
      acc = mfma(K[2 * kg], qf[0], acc);
      acc = mfma(K[2 * kg + 1], qf[1], acc);
      float v = b1 ? (b0 ? acc[3] : acc[2]) : (b0 ? acc[1] : acc[0]);
      v += dpp<XOR1>(v); v += dpp<XOR2>(v);
      s = (mykg == kg) ? v : s;
    }
    s = rbf(s) * 0.0625f;
    float m = s;
    m = fmaxf(m, dpp<XOR1>(m)); m = fmaxf(m, dpp<XOR2>(m)); m = fmaxf(m, dpp<ROR8>(m)); m = mx16(m); m = mx32(m);
    if ((lane & ~4) == 0) ldf[L_MW + wave * 2 + (lane >> 2)] = m;
    LGKM0();
    if (lane == 0) LDS_INC(F_MC);
    while (flg[F_MC] < 8u) __builtin_amdgcn_s_sleep(1);
    CBAR();
    {
      const int head = (lane >> 2) & 1;
      float mm = ldf[L_MW + head];
#pragma unroll
      for (int w = 1; w < 8; ++w) mm = fmaxf(mm, ldf[L_MW + 2 * w + head]);
      m = mm;
    }
    const float p = __builtin_amdgcn_exp2f((s - m) * 1.4426950408889634f);
    const unsigned pb = f2bf(p);
    lds[L_P + wave * 64 + ((lane >> 2) & 1) * 32 + (lane >> 4) * 8 + mykg] = pb | (pb << 16);
    float l = asf(pb << 16);
    l += dpp<XOR1>(l); l += dpp<XOR2>(l); l += dpp<ROR8>(l); l = tr16(l, l); l = tr32(l, l);
    if ((lane & ~4) == 0) ldf[L_LW + wave * 2 + (lane >> 2)] = l;
    LGKM0();
    unsigned old = 0;
    if (lane == 0) { flg[F_P0 + wave] = 1u; old = LDS_INC(F_LC); }
    old = __builtin_amdgcn_readfirstlane(old);
    if (old == 7u) {
      while (flg[F_T] == 0u) __builtin_amdgcn_s_sleep(1);
      CBAR();
      __builtin_amdgcn_s_setprio(3);
      if ((lane & ~4) == 0) {
        const int head = lane >> 2;
        float ls = 0.f;
#pragma unroll
        for (int w = 0; w < 8; ++w) ls += ldf[L_LW + 2 * w + head];
        v2u rec = {enc(m), enc(ls)};
        __builtin_amdgcn_raw_buffer_store_b64(rec, rs, (int)(WS_ML + ((2 * g + head) * 32 + j) * 8), 0, AUX_SC1);
      }
      __builtin_amdgcn_s_setprio(0);
    }
    if (wave == 0) STAMP(4);
    asm volatile("" :: "v"(cpf));
    if (t3a) {
      float a[8];
  #pragma unroll
      for (int c = 0; c < 4; ++c) { a[2 * c] = bflo(wgu_r[c]); a[2 * c + 1] = bfhi(wgu_r[c]); }
      const float z = red8_b345(a, b3);
      ldf[L_CSP + wave * 64 + 8 * (lane & 7) + 4 * lb5 + 2 * lb4 + (lane >> 3 & 1)] = z;
      LGKM0();
      if (lane == 0) LDS_INC(F_CSC);
      if (wave == 4 || wave == 5) {
        while (flg[F_CSC] < 16u || flg[F_T] == 0u) __builtin_amdgcn_s_sleep(4);
        CBAR();
        float cs_ = 0.f;
  #pragma unroll
        for (int w = 0; w < 8; ++w) cs_ += ldf[L_CSP + ((wave - 4) * 8 + w) * 64 + lane];
        __builtin_amdgcn_raw_buffer_store_b32(enc(cs_), rs, (int)(WS_CS + (cb * 7 + rb) * 512 + (wave - 4) * 256 + lane * 4), 0, AUX_SC1);
      }
    }

    if (wave == 7) {
      while (flg[F_T] == 0u) __builtin_amdgcn_s_sleep(2);
      CBAR();
      __builtin_amdgcn_s_setprio(3);
      const unsigned t0 = lds[L_TAG], t1 = lds[L_TAG + 1];
      unsigned spins = 0;
      for (;;) {
        CBAR();
        const v4u r0 = __builtin_amdgcn_raw_buffer_load_b128(rs, (int)(WS_READY + lane * 16), 0, AUX_SC1);
        const v4u r1 = __builtin_amdgcn_raw_buffer_load_b128(rs, (int)(WS_READY + 1024 + lane * 16), 0, AUX_SC1);
        const bool ok = (r0[0] == t0) & (r0[1] == t1) & (r0[2] == t0) & (r0[3] == t1) & (r1[0] == t0) & (r1[1] == t1) & (r1[2] == t0) & (r1[3] == t1);
        if (__all(ok) || ++spins > SPIN_CAP) break;
        __builtin_amdgcn_s_sleep(8);
      }
      if (lane == 0) {
        flg[F_R] = 1u;
        if (blockIdx.x == 0) __builtin_amdgcn_raw_buffer_store_b64(v2u{t0, t1}, rs, (int)WS_E, 0, AUX_SC1);
      }
      if (t2) {
        v2u rec = {1u, 1u};
        bool ok = lane >= 32;
        spins = 0;
        for (;;) {
          CBAR();
          if (!ok) { rec = __builtin_amdgcn_raw_buffer_load_b64(rs, (int)(WS_ML + (hq * 32 + lane) * 8), 0, AUX_SC1); ok = (rec[0] != 0u) & (rec[1] != 0u); }
          if (__all(ok) || ++spins > SPIN_CAP) break;
          __builtin_amdgcn_s_sleep(8);
        }
        const float ms = lane < 32 ? dec(rec[0]) : -INFINITY, lsum = lane < 32 ? dec(rec[1]) : 0.f;
        const float M = wave_max(ms);
        const float e = lane < 32 ? __builtin_amdgcn_exp2f((ms - M) * 1.4426950408889634f) : 0.f;
        const float L = wave_sum(e * lsum);
        if (lane < 32) ldf[L_WTS + lane] = e / L;
        LGKM0();
        if (lane == 0) flg[F_W2] = 1u;
      }
      __builtin_amdgcn_s_setprio(0);
      STAMP(5);
    }
    if (wave == 6 && t3b) {
      while (flg[F_R] == 0u) __builtin_amdgcn_s_sleep(4);
      const int o0 = (int)(WS_CS + ((fidx >> 3) * 7 + (lane >> 4)) * 512 + (((lane >> 3) & 1) * 64 + 8 * (fidx & 7) + (lane & 7)) * 4);
      unsigned c0 = 0u, c1 = lane < 48 ? 0u : 0x80000000u;
      unsigned spins = 0;
      for (;;) {
        CBAR();
        if (c0 == 0u) c0 = __builtin_amdgcn_raw_buffer_load_b32(rs, o0, 0, AUX_SC1);
        if (c1 == 0u) c1 = __builtin_amdgcn_raw_buffer_load_b32(rs, o0 + 2048, 0, AUX_SC1);
        if (__all((c0 != 0u) & (c1 != 0u)) || ++spins > SPIN_CAP) break;
        __builtin_amdgcn_s_sleep(16);
      }
      float cs_ = dec(c0) + dec(c1);
      cs_ = tr16(cs_, cs_); cs_ = tr32(cs_, cs_);
      if (lane < 16) ldf[L_CST + lane] = cs_;
      LGKM0();
      if (lane == 0) flg[F_CS] = 1u;
    }
  } else {
    const int v = wave - 8;
    __builtin_amdgcn_s_setprio(3);
    const v2u ev = __builtin_amdgcn_raw_buffer_load_b64(rs, (int)WS_E, 0, AUX_SC1);
    if (v < 7) {
      const int r0 = 112 * (j & 3) + 16 * v + (lane >> 3);
      const v4u xv = *(const v4u*)(x + 8 * (lane < 56 ? lane : 55));
      const unsigned xr0 = (unsigned)x[r0], xr1 = (unsigned)x[r0 + 8];
      const u16* wqp = wq + (size_t)r0 * 4096 + 64 * (8 * g + (j >> 2)) + 8 * (lane & 7);
      const v4u W0 = ldnt(wqp), W1 = ldnt(wqp + (size_t)8 * 4096);
      CBAR();
      if (lane == 0) LDS_INC(F_EI);
      CBAR();
      float xf[8];
#pragma unroll
      for (int i = 0; i < 4; ++i) { xf[2 * i] = bflo(xv[i]); xf[2 * i + 1] = bfhi(xv[i]); }
      float sm = ((xf[0] + xf[1]) + (xf[2] + xf[3])) + ((xf[4] + xf[5]) + (xf[6] + xf[7]));
      sm = lane < 56 ? sm : 0.f;
      const float mean = wave_sum(sm) * (1.f / 448.f);
      float vs = 0.f;
#pragma unroll
      for (int i = 0; i < 8; ++i) { const float d = xf[i] - mean; vs += d * d; }
      vs = lane < 56 ? vs : 0.f;
      const float var = wave_sum(vs) * (1.f / 448.f);
      const float mb = rbf(mean), den = rbf(__builtin_sqrtf(rbf(rbf(var) + 1e-5f)));
      const float xn0 = rbf(rbf(bflo(xr0) - mb) / den), xn1 = rbf(rbf(bflo(xr1) - mb) / den);
      float acc[8];
#pragma unroll
      for (int c = 0; c < 4; ++c) {
        acc[2 * c] = fmaf(xn1, bflo(W1[c]), xn0 * bflo(W0[c]));
        acc[2 * c + 1] = fmaf(xn1, bfhi(W1[c]), xn0 * bfhi(W0[c]));
      }
      const float qv = red8_b345(acc, b3);
      ldf[L_QP + v * 64 + 8 * (lane & 7) + 4 * lb5 + 2 * lb4 + (lane >> 3 & 1)] = qv;
      LGKM0();
      unsigned oldq = 0;
      if (lane == 0) oldq = LDS_INC(F_QC);
      oldq = __builtin_amdgcn_readfirstlane(oldq);
      if (oldq == 6u) {
        CBAR();
        const u64 T = (((u64)ev[1] << 32) | ev[0]) + 1ull;
        float qs = 0.f;
#pragma unroll
        for (int w = 0; w < 7; ++w) qs += ldf[L_QP + w * 64 + lane];
        const unsigned qbits = (asu(qs) + 0x80u) & 0xffffff00u;
        v2u gr = {qbits | ((unsigned)T & 0xffu), (unsigned)(T >> 8)};
        __builtin_amdgcn_raw_buffer_store_b64(gr, rs, (int)(WS_QG + (8 * g + (j >> 2)) * 2048 + (j & 3) * 512 + lane * 8), 0, AUX_SC1);
        if (lane == 0) { lds[L_TAG] = (unsigned)T; lds[L_TAG + 1] = (unsigned)(T >> 32); LGKM0(); flg[F_QP] = 1u; }
        STAMP(1);
      }
    } else {
      const v4u z4 = {0u, 0u, 0u, 0u};
      __builtin_amdgcn_raw_buffer_store_b128(z4, rs, (int)(WS_PART + ((2 * g) * 32 + j) * 1024 + 16 * lane), 0, AUX_SC1);
      __builtin_amdgcn_raw_buffer_store_b128(z4, rs, (int)(WS_PART + ((2 * g + 1) * 32 + j) * 1024 + 16 * lane), 0, AUX_SC1);
      if (lane < 2) __builtin_amdgcn_raw_buffer_store_b64(v2u{0u, 0u}, rs, (int)(WS_ML + ((2 * g + lane) * 32 + j) * 8), 0, AUX_SC1);
      if (t2 && lane < 16) __builtin_amdgcn_raw_buffer_store_b128(z4, rs, (int)(WS_Y + (cb7 * 32 + hq * 2 + rh) * 256 + 16 * lane), 0, AUX_SC1);
      if (t3a) {
        if (lane < 32) __builtin_amdgcn_raw_buffer_store_b128(z4, rs, (int)(WS_CS + (cb * 7 + rb) * 512 + 16 * lane), 0, AUX_SC1);
        __builtin_amdgcn_raw_buffer_store_b128(z4, rs, (int)(WS_G + (cb * 7 + rb) * 1024 + 16 * lane), 0, AUX_SC1);
      }
      if (t3b) {
        __builtin_amdgcn_raw_buffer_store_b128(z4, rs, (int)(WS_F + fidx * 1792 + 16 * lane), 0, AUX_SC1);
        if (lane < 48) __builtin_amdgcn_raw_buffer_store_b128(z4, rs, (int)(WS_F + fidx * 1792 + 1024 + 16 * lane), 0, AUX_SC1);
      }
      CBAR();
      if (lane == 0) LDS_INC(F_EI);
      VM0();
      const u64 T = (((u64)ev[1] << 32) | ev[0]) + 1ull;
      if (lane == 0) {
        __builtin_amdgcn_raw_buffer_store_b64(v2u{(unsigned)T, (unsigned)(T >> 32)}, rs, (int)(WS_READY + blockIdx.x * 8), 0, AUX_SC1);
        flg[F_T] = 1u;
      }
      STAMP(3);
    }
    __builtin_amdgcn_s_setprio(0);
    const u16* vb = vc + ((size_t)g * 8192 + 256 * j + 32 * v + (lane >> 4)) * 256 + 8 * (lane & 15);
    v4u V[16];
    while (flg[F_KI0 + v] == 0u) __builtin_amdgcn_s_sleep(1);
    CBAR();
    if (t2) wo_r = ldnt(wo_p);
    if (t3a) wgu_r = ldnt(wgu_p);
    if (t3b && v < 2) {
      const int tc = (tid - 512) < 112 ? (tid - 512) : 111;
#pragma unroll
      for (int r = 0; r < 8; ++r) wd_r[r] = __builtin_nontemporal_load((const v2u*)(wd + (size_t)(8 * fidx + r) * 448 + 4 * tc));
    }
    CBAR();
#pragma unroll
    for (int i = 0; i < 16; ++i) { V[i] = ldnt(vb + (i >> 1) * 1024 + (i & 1) * 128); CBAR(); }
    __builtin_amdgcn_s_setprio(2);
    if (t3a) {
      float a[8];
#pragma unroll
      for (int c = 0; c < 4; ++c) { a[2 * c] = bflo(wgu_r[c]); a[2 * c + 1] = bfhi(wgu_r[c]); }
      const float z = red8_b345(a, b3);
      ldf[L_CSP + wave * 64 + 8 * (lane & 7) + 4 * lb5 + 2 * lb4 + (lane >> 3 & 1)] = z;
      LGKM0();
      if (lane == 0) LDS_INC(F_CSC);
    }
    while (flg[F_P0 + v] == 0u) __builtin_amdgcn_s_sleep(2);
    CBAR();
    const LAS v4u* pp = (const LAS v4u*)(lds + L_P + v * 64 + ((lane >> 3) & 1) * 32 + (lane >> 4) * 8);
    const v4u pa = pp[0], pb = pp[1];
    const unsigned hm = (lane & 1) ? 0xffff0000u : 0x0000ffffu;
    const int rsel = (lane >> 1) & 3;
    const unsigned m0 = rsel == 0 ? hm : 0u, m1 = rsel == 1 ? hm : 0u, m2 = rsel == 2 ? hm : 0u, m3 = rsel == 3 ? hm : 0u;
    f32x4 a0 = {0.f, 0.f, 0.f, 0.f}, a1 = {0.f, 0.f, 0.f, 0.f};
#pragma unroll
    for (int kg = 0; kg < 8; ++kg) {
      const unsigned pd = kg < 4 ? pa[kg & 3] : pb[kg & 3];
      const v4u A = {pd & m0, pd & m1, pd & m2, pd & m3};
      a0 = mfma(A, V[2 * kg], a0);
      a1 = mfma(A, V[2 * kg + 1], a1);
    }
    LAS f32x4* rp = (LAS f32x4*)(ldf + L_RED + v * 512 + (lane >> 5) * 256 + 8 * (lane & 15) + 4 * ((lane >> 4) & 1));
    rp[0] = a0; rp[32] = a1;
    LGKM0();
    unsigned oldv = 0;
    if (lane == 0) oldv = LDS_INC(F_VC);
    oldv = __builtin_amdgcn_readfirstlane(oldv);
    if (oldv == 7u) {
      CBAR();
      STAMP(6);
      const LAS f32x4* sp = (const LAS f32x4*)(ldf + L_RED + 4 * lane);
      f32x4 r0[8], r1[8];
#pragma unroll
      for (int w = 0; w < 8; ++w) { r0[w] = sp[w * 128]; r1[w] = sp[w * 128 + 64]; }
      const f32x4 s0 = ((r0[0] + r0[1]) + (r0[2] + r0[3])) + ((r0[4] + r0[5]) + (r0[6] + r0[7]));
      const f32x4 s1 = ((r1[0] + r1[1]) + (r1[2] + r1[3])) + ((r1[4] + r1[5]) + (r1[6] + r1[7]));
      v4u e0 = {enc(s0[0]), enc(s0[1]), enc(s0[2]), enc(s0[3])};
      v4u e1 = {enc(s1[0]), enc(s1[1]), enc(s1[2]), enc(s1[3])};
      __builtin_amdgcn_raw_buffer_store_b128(e0, rs, (int)(WS_PART + ((2 * g) * 32 + j) * 1024 + 16 * lane), 0, AUX_SC1);
      __builtin_amdgcn_raw_buffer_store_b128(e1, rs, (int)(WS_PART + ((2 * g + 1) * 32 + j) * 1024 + 16 * lane), 0, AUX_SC1);
      STAMP(7);
    }
  }
  if (t2) {
    {
      while (flg[F_VC] < 8u || flg[F_R] == 0u) __builtin_amdgcn_s_sleep(1);
      CBAR();
      v4u pa;
      unsigned spins = 0;
      const int off = (int)(WS_PART + (hq * 32 + (lane >> 1)) * 1024 + (128 * rh + 8 * wave + 4 * (lane & 1)) * 4);
      bool ok = false;
      __builtin_amdgcn_s_sleep(T2DELAY);
      for (;;) {
        CBAR();
        if (!ok) {
          pa = __builtin_amdgcn_raw_buffer_load_b128(rs, off, 0, AUX_SC1);
          ok = (pa[0] != 0u) & (pa[1] != 0u) & (pa[2] != 0u) & (pa[3] != 0u);
        }
        if (__all(ok) || ++spins > SPIN_CAP) break;
        __builtin_amdgcn_s_sleep(PSLEEP);
      }
      if (wave == 0) STAMP(8);
      if (wave == 15) STAMP(21);
      if (wave == 7) STAMP(22);
      while (flg[F_W2] == 0u) __builtin_amdgcn_s_sleep(1);
      CBAR();
      const float w = ldf[L_WTS + (lane >> 1)];
      const float u0 = tr32(w * dec(pa[0]), w * dec(pa[2])), u1 = tr32(w * dec(pa[1]), w * dec(pa[3]));
      float o = tr16(u0, u1);
      o += dpp<XOR2>(o); o += dpp<ROR8>(o); o += dpp<ROR4>(o);
      o = rbf(o);
      float y[8];
#pragma unroll
      for (int c = 0; c < 4; ++c) { y[2 * c] = o * bflo(wo_r[c]); y[2 * c + 1] = o * bfhi(wo_r[c]); }
#pragma unroll
      for (int c = 0; c < 4; ++c) y[c] = tr32(y[c], y[c + 4]);
#pragma unroll
      for (int c = 0; c < 2; ++c) y[c] = tr16(y[c], y[c + 2]);
      const float yy = trd<XOR1>(y[0], y[1], b0);
      ldf[L_RED2 + wave * 64 + 8 * ((lane >> 1) & 7) + 4 * lb5 + 2 * lb4 + (lane & 1)] = yy;
    }
    __syncthreads();
    if (wave == 0) {
      STAMP(18);
      const LAS f32x4* rp = (const LAS f32x4*)(ldf + L_RED2 + (lane >> 4) * 256 + 4 * (lane & 15));
      const f32x4 a = (rp[0] + rp[16]) + (rp[32] + rp[48]);
      const float u0 = tr32(a[0], a[2]), u1 = tr32(a[1], a[3]);
      const float z = tr16(u0, u1);
      __builtin_amdgcn_raw_buffer_store_b32(enc(z), rs, (int)(WS_Y + (cb7 * 32 + hq * 2 + rh) * 256 + (4 * (lane & 15) + 2 * lb5 + lb4) * 4), 0, AUX_SC1);
      STAMP(9);
    }
  }
  if (t3a) {
    const unsigned xraw = (unsigned)x[64 * rb + 8 * (wave & 7) + (lane >> 3)];
    if (wave >= 8) {
      v4u yv;
      unsigned spins = 0;
      bool ok = false;
      const int off = (int)(WS_Y + (rb * 32 + 4 * (wave - 8) + (lane >> 4)) * 256 + (lane & 15) * 16);
      __builtin_amdgcn_s_sleep(T3DELAY);
      for (;;) {
        CBAR();
        if (!ok) {
          yv = __builtin_amdgcn_raw_buffer_load_b128(rs, off, 0, AUX_SC1);
          ok = (yv[0] != 0u) & (yv[1] != 0u) & (yv[2] != 0u) & (yv[3] != 0u);
        }
        if (__all(ok) || ++spins > SPIN_CAP) break;
        __builtin_amdgcn_s_sleep(PSLEEP);
      }
      if (wave == 8) STAMP(10);
      if (wave == 15) STAMP(19);
      if (wave == 12) STAMP(20);
      const float u0 = tr32(dec(yv[0]), dec(yv[2])), u1 = tr32(dec(yv[1]), dec(yv[3]));
      ldf[L_HP + (wave - 8) * 64 + 4 * (lane & 15) + 2 * lb5 + lb4] = tr16(u0, u1);
    }
    __syncthreads();
    if (wave == 0) STAMP(14);
    {
      const int row = 8 * (wave & 7) + (lane >> 3);
      float t8[8];
#pragma unroll
      for (int w = 0; w < 8; ++w) t8[w] = ldf[L_HP + w * 64 + row];
      const float at = ((t8[0] + t8[1]) + (t8[2] + t8[3])) + ((t8[4] + t8[5]) + (t8[6] + t8[7]));
      const float h = rbf(bflo(xraw) + rbf(at));
      float a[8];
#pragma unroll
      for (int c = 0; c < 4; ++c) { a[2 * c] = h * bflo(wgu_r[c]); a[2 * c + 1] = h * bfhi(wgu_r[c]); }
      const float z = red8_b345(a, b3);
      ldf[L_RED3 + wave * 64 + 8 * (lane & 7) + 4 * lb5 + 2 * lb4 + (lane >> 3 & 1)] = z;
      if (wave < 8 && (lane & 7) == 0) ldf[L_QP + row] = h;
    }
    __syncthreads();
    if (wave == 0) STAMP(15);
    if (wave < 2) {
      float t8[8];
#pragma unroll
      for (int w = 0; w < 8; ++w) t8[w] = ldf[L_RED3 + (wave * 8 + w) * 64 + lane];
      const float z = ((t8[0] + t8[1]) + (t8[2] + t8[3])) + ((t8[4] + t8[5]) + (t8[6] + t8[7]));
      __builtin_amdgcn_raw_buffer_store_b32(enc(z), rs, (int)(WS_G + (cb * 7 + rb) * 1024 + (lane >> 3) * 128 + (8 * wave + (lane & 7)) * 4), 0, AUX_SC1);
    } else if (wave == 2) {
      const float h = ldf[L_QP + lane];
      const float piv = asf((unsigned)__builtin_amdgcn_readfirstlane((int)asu(h)));
      const float d = h - piv;
      float st = tr32(d, d * d);
      st = tr16(st, st); st += dpp<XOR1>(st); st += dpp<XOR2>(st); st += dpp<ROR8>(st); st += dpp<ROR4>(st);
      const int k = lane >> 3;
      if (k == 0 || k == 1 || k == 4) {
        const float val = k == 0 ? piv : st;
        __builtin_amdgcn_raw_buffer_store_b32(enc(val), rs, (int)(WS_G + (cb * 7 + rb) * 1024 + (lane & 7) * 128 + (16 + (k == 4 ? 2 : k)) * 4), 0, AUX_SC1);
      }
    }
    if (wave == 0) STAMP(16);
  }
  if (t3b) {
    if (wave == 8 || wave == 9) {
      if (!t3a) { while (flg[F_VC] < 8u || flg[F_R] == 0u) __builtin_amdgcn_s_sleep(1); __builtin_amdgcn_s_sleep(40); }
      v4u gv = {1u, 1u, 1u, 1u};
      bool ok = (lane >= 56) || ((lane & 7) > 4);
      unsigned spins = 0;
      const int off = (int)(WS_G + ((fidx >> 3) * 7 + (lane >> 3)) * 1024 + (fidx & 7) * 128 + (lane & 7) * 16);
      __builtin_amdgcn_s_sleep(T3BDELAY);
      for (;;) {
        CBAR();
        if (!ok) {
          gv = __builtin_amdgcn_raw_buffer_load_b128(rs, off, 0, AUX_SC1);
          ok = (gv[0] != 0u) & (gv[1] != 0u) & (gv[2] != 0u) & ((gv[3] != 0u) | ((lane & 7) == 4));
        }
        if (__all(ok) || ++spins > SPIN_CAP) break;
        __builtin_amdgcn_s_sleep(PSLEEP);
      }
      if (wave == 8) STAMP(17);
      while (flg[F_CS] == 0u) __builtin_amdgcn_s_sleep(1);
      CBAR();
      const bool live = lane < 56;
      const bool isst = (lane & 7) == 4;
      const float piv = dec(gv[0]), s1 = dec(gv[1]), s2 = dec(gv[2]);
      const float dm = s1 * (1.f / 64.f);
      const float mw = piv + dm, m2 = s2 - s1 * dm;
      float a0 = live ? (isst ? mw : dec(gv[0])) : 0.f, a1 = live ? dec(gv[1]) : 0.f, a2 = live ? dec(gv[2]) : 0.f, a3 = live ? dec(gv[3]) : 0.f;
      a0 = tr32(a0, a0); a1 = tr32(a1, a1); a2 = tr32(a2, a2); a3 = tr32(a3, a3);
      a0 = tr16(a0, a0); a1 = tr16(a1, a1); a2 = tr16(a2, a2); a3 = tr16(a3, a3);
      a0 += dpp<ROR8>(a0); a1 += dpp<ROR8>(a1); a2 += dpp<ROR8>(a2); a3 += dpp<ROR8>(a3);
      const float mean = asf((unsigned)__builtin_amdgcn_readlane((int)asu(a0), 4)) * (1.f / 7.f);
      const float dd = mw - mean;
      float vv = (live && isst) ? m2 + 64.f * dd * dd : 0.f;
      vv = tr32(vv, vv); vv = tr16(vv, vv); vv += dpp<ROR8>(vv);
      const float var = fmaxf(asf((unsigned)__builtin_amdgcn_readlane((int)asu(vv), 4)) * (1.f / 448.f), 0.f);
      const float mb = rbf(mean), den = rbf(__builtin_sqrtf(rbf(rbf(var) + 1e-5f)));
      const float rden = 1.f / den;
      const f32x4 cs4 = *(const LAS f32x4*)(ldf + L_CST + 4 * (lane & 3));
      const float z0 = rbf((a0 - mb * cs4[0]) * rden), z1 = rbf((a1 - mb * cs4[1]) * rden), z2 = rbf((a2 - mb * cs4[2]) * rden), z3 = rbf((a3 - mb * cs4[3]) * rden);
      const float u0 = dpp<XOR2>(z0), u1 = dpp<XOR2>(z1), u2 = dpp<XOR2>(z2), u3 = dpp<XOR2>(z3);
      float av[4];
      {
        const float zz[4] = {z0, z1, z2, z3}, uu[4] = {u0, u1, u2, u3};
#pragma unroll
        for (int i = 0; i < 4; ++i) {
          const float z = zz[i];
          const float t = 0.7978845608028654f * (z + 0.044715f * z * z * z);
          const float e2 = __builtin_amdgcn_exp2f(t * 2.8853900817779268f);
          const float th = 1.f - 2.f * __builtin_amdgcn_rcpf(e2 + 1.f);
          av[i] = rbf(rbf(0.5f * z * (1.f + th)) * uu[i]);
        }
      }
      f32x4 f = {0.f, 0.f, 0.f, 0.f};
#pragma unroll
      for (int r = 0; r < 8; ++r) {
        const float ar = asf((unsigned)__builtin_amdgcn_readlane((int)asu(av[r & 3]), r >> 2));
        f[0] = fmaf(ar, bflo(wd_r[r][0]), f[0]); f[1] = fmaf(ar, bfhi(wd_r[r][0]), f[1]);
        f[2] = fmaf(ar, bflo(wd_r[r][1]), f[2]); f[3] = fmaf(ar, bfhi(wd_r[r][1]), f[3]);
      }
      if (tid - 512 < 112) {
        v4u e = {enc(f[0]), enc(f[1]), enc(f[2]), enc(f[3])};
        __builtin_amdgcn_raw_buffer_store_b128(e, rs, (int)(WS_F + fidx * 1792 + 16 * (tid - 512)), 0, AUX_SC1);
      }
      if (wave == 8) STAMP(11);
    }
  } else if (t4) {
    const int rid = j;
    {
      const int off = wave < 14 ? (int)(WS_F + (16 * wave + (lane >> 2)) * 1792 + (16 * rid + 4 * (lane & 3)) * 4)
                                : (int)(WS_Y + ((rid >> 2) * 32 + 16 * (wave - 14) + (lane >> 2)) * 256 + (16 * (rid & 3) + 4 * (lane & 3)) * 4);
      v4u pv;
      unsigned spins = 0;
      bool ok = false;
      if (wave < 14) { while (flg[F_Y4] < 2u) __builtin_amdgcn_s_sleep(1); __builtin_amdgcn_s_sleep(T4DELAY); }
      else __builtin_amdgcn_s_sleep(T3DELAY);
      for (;;) {
        CBAR();
        if (!ok) {
          pv = __builtin_amdgcn_raw_buffer_load_b128(rs, off, 0, AUX_SC1);
          ok = (pv[0] != 0u) & (pv[1] != 0u) & (pv[2] != 0u) & (pv[3] != 0u);
        }
        if (__all(ok) || ++spins > SPIN_CAP) break;
        __builtin_amdgcn_s_sleep(PSLEEP);
      }
      if (wave >= 14 && lane == 0) LDS_INC(F_Y4);
      if (wave == 0) STAMP(12);
      const float a0 = tr32(dec(pv[0]), dec(pv[2])), a1 = tr32(dec(pv[1]), dec(pv[3]));
      float z = tr16(a0, a1);
      z += dpp<ROR8>(z); z += dpp<ROR4>(z);
      const int col = 4 * (lane & 3) + 2 * lb5 + lb4;
      if ((lane & 12) == 0) ldf[(wave < 14 ? L_RED4 + wave * 16 : L_H4 + (wave - 14) * 16) + col] = z;
    }
    const unsigned xraw = (unsigned)x[16 * rid + (tid & 15)];
    __syncthreads();
    if (tid < 16) {
      float t[14];
#pragma unroll
      for (int w = 0; w < 14; ++w) t[w] = ldf[L_RED4 + w * 16 + tid];
      const float f = (((t[0] + t[1]) + (t[2] + t[3])) + ((t[4] + t[5]) + (t[6] + t[7]))) + (((t[8] + t[9]) + (t[10] + t[11])) + (t[12] + t[13]));
      const float h = rbf(bflo(xraw) + rbf(ldf[L_H4 + tid] + ldf[L_H4 + 16 + tid]));
      out[16 * rid + tid] = (u16)f2bf(h + rbf(f));
    }
    if (wave == 0) STAMP(13);
  }
}

extern "C" void kernel_launch(void* const* d_in, const int* in_sizes, int n_in,
                              void* d_out, int out_size, void* d_ws, size_t ws_size,
                              hipStream_t stream) {
  fused_decode<<<256, 1024, 0, stream>>>((const u16*)d_in[0], (const u16*)d_in[1], (const u16*)d_in[2], (const u16*)d_in[3],
                                         (const u16*)d_in[4], (const u16*)d_in[6], (const u16*)d_in[7], (const u16*)d_in[8],
                                         (u16*)d_out, (unsigned char*)d_ws);
}
```

```cpp
#include <hip/hip_runtime.h>
#include <stdint.h>

typedef unsigned short u16;
typedef unsigned long long u64;
typedef unsigned v4u __attribute__((ext_vector_type(4)));
typedef unsigned v2u __attribute__((ext_vector_type(2)));
typedef float f32x4 __attribute__((ext_vector_type(4)));
typedef short bf16x8 __attribute__((ext_vector_type(8)));
#define LAS __attribute__((address_space(3)))

#define STAMPS 0

constexpr int AUX_SC1 = 16;
constexpr unsigned WS_E = 0;
constexpr unsigned WS_READY = 4096;
constexpr unsigned WS_QG = 8192;
constexpr unsigned WS_ML = 139264;
constexpr unsigned WS_Y = 143360;
constexpr unsigned WS_CS = 200704;
constexpr unsigned WS_G = 301056;
constexpr unsigned WS_PART = 501760;
constexpr unsigned WS_F = 1026048;
constexpr unsigned WS_END = 1427456;
constexpr unsigned WS_STAMP = 2u << 20;
constexpr unsigned SPIN_CAP = 1u << 17;
#define CODEPF 1
#define CODEBYTES 13824
#define TLBPF 1
#define QDELAY 16
#define T2DELAY 8
#define T3DELAY 8
#define T3BDELAY 8
#define T4DELAY 40
#define PSLEEP 4

__device__ __forceinline__ float asf(unsigned u) { return __builtin_bit_cast(float, u); }
__device__ __forceinline__ unsigned asu(float f) { return __builtin_bit_cast(unsigned, f); }
__device__ __forceinline__ float bflo(unsigned w) { return asf(w << 16); }
__device__ __forceinline__ float bfhi(unsigned w) { return asf(w & 0xffff0000u); }
__device__ __forceinline__ unsigned f2bf(float f) { __bf16 b = (__bf16)f; return (unsigned)__builtin_bit_cast(u16, b); }
__device__ __forceinline__ float rbf(float f) { return asf(f2bf(f) << 16); }
__device__ __forceinline__ unsigned enc(float f) { unsigned u = asu(f); u = (u == 0x80000000u) ? 0u : u; return u ^ 0x80000000u; }
__device__ __forceinline__ float dec(unsigned u) { return asf(u ^ 0x80000000u); }

template <int CTRL> __device__ __forceinline__ float dpp(float x) {
  return asf((unsigned)__builtin_amdgcn_mov_dpp((int)asu(x), CTRL, 0xf, 0xf, true));
}
constexpr int XOR1 = 0xB1, XOR2 = 0x4E, ROR4 = 0x124, ROR8 = 0x128;
__device__ __forceinline__ float tr32(float a, float b) {
  auto r = __builtin_amdgcn_permlane32_swap(asu(a), asu(b), false, false);
  return asf(r[0]) + asf(r[1]);
}
__device__ __forceinline__ float tr16(float a, float b) {
  auto r = __builtin_amdgcn_permlane16_swap(asu(a), asu(b), false, false);
  return asf(r[0]) + asf(r[1]);
}
template <int CTRL> __device__ __forceinline__ float trd(float a, float b, bool bit) {
  const float t = bit ? a : b, k = bit ? b : a;
  return k + dpp<CTRL>(t);
}
__device__ __forceinline__ float mx32(float a) {
  auto r = __builtin_amdgcn_permlane32_swap(asu(a), asu(a), false, false);
  return fmaxf(asf(r[0]), asf(r[1]));
}
__device__ __forceinline__ float mx16(float a) {
  auto r = __builtin_amdgcn_permlane16_swap(asu(a), asu(a), false, false);
  return fmaxf(asf(r[0]), asf(r[1]));
}
__device__ __forceinline__ float wave_sum(float x) {
  x += dpp<XOR1>(x); x += dpp<XOR2>(x); x += dpp<ROR8>(x); x += dpp<ROR4>(x);
  x = tr16(x, x); x = tr32(x, x);
  return x;
}
__device__ __forceinline__ float wave_max(float x) {
  x = fmaxf(x, dpp<XOR1>(x)); x = fmaxf(x, dpp<XOR2>(x)); x = fmaxf(x, dpp<ROR8>(x)); x = fmaxf(x, dpp<ROR4>(x));
  x = mx16(x); x = mx32(x);
  return x;
}
__device__ __forceinline__ float red8_b345(float (&a)[8], bool b3) {
#pragma unroll
  for (int c = 0; c < 4; ++c) a[c] = tr32(a[c], a[c + 4]);
#pragma unroll
  for (int c = 0; c < 2; ++c) a[c] = tr16(a[c], a[c + 2]);
  return trd<ROR8>(a[0], a[1], b3);
}
#define KVPOL 1
__device__ __forceinline__ v4u ldnt(const u16* p) {
  return __builtin_nontemporal_load((const v4u*)p);
}
__device__ __forceinline__ f32x4 mfma(v4u a, v4u b, f32x4 c) {
  return __builtin_amdgcn_mfma_f32_16x16x32_bf16(__builtin_bit_cast(bf16x8, a), __builtin_bit_cast(bf16x8, b), c, 0, 0, 0);
}
#define LGKM0() asm volatile("s_waitcnt lgkmcnt(0)" ::: "memory")
#define VM0() asm volatile("s_waitcnt vmcnt(0)" ::: "memory")
#define CBAR() asm volatile("" ::: "memory")
#define LDS_INC(f) __hip_atomic_fetch_add((LAS unsigned*)(lds + L_FLAG + (f)), 1u, __ATOMIC_RELAXED, __HIP_MEMORY_SCOPE_WORKGROUP)

constexpr int L_Q = 0;
constexpr int L_QP = 256;
constexpr int L_P = 704;
constexpr int L_MW = 1216;
constexpr int L_LW = 1232;
constexpr int L_WTS = 1248;
constexpr int L_FLAG = 1280;
constexpr int L_TAG = 1312;
constexpr int L_CST = 1328;
constexpr int L_RED2 = 1344;
constexpr int L_HP = 2368;
constexpr int L_RED3 = 2880;
constexpr int L_CSP = 3904;
constexpr int L_RED4 = 4928;
constexpr int L_H4 = 5152;
constexpr int L_RED = 5184;
constexpr int L_TOTAL = L_RED + 4096;
constexpr int F_QD = 0, F_R = 1, F_MC = 2, F_LC = 3, F_VC = 4, F_T = 5, F_QC = 6, F_EI = 7, F_P0 = 8, F_KI0 = 16,
              F_Y4 = 24, F_W2 = 25, F_QP = 26, F_CSC = 27, F_CS = 28;

__global__ void __launch_bounds__(1024) fused_decode(const u16* __restrict__ x, const u16* __restrict__ kc, const u16* __restrict__ vc,
                                                     const u16* __restrict__ wq, const u16* __restrict__ wo, const u16* __restrict__ wg,
                                                     const u16* __restrict__ wu, const u16* __restrict__ wd, u16* __restrict__ out,
                                                     unsigned char* __restrict__ ws) {
  __shared__ __attribute__((aligned(16))) unsigned lds_raw[L_TOTAL];
  LAS unsigned* lds = (LAS unsigned*)lds_raw;
  LAS float* ldf = (LAS float*)lds_raw;
  volatile LAS unsigned* flg = (volatile LAS unsigned*)(lds + L_FLAG);
  const int tid = threadIdx.x, lane = tid & 63, wave = __builtin_amdgcn_readfirstlane(tid >> 6);
  const int g = blockIdx.x & 7, j = blockIdx.x >> 3;
  const bool t2 = j < 28;
  const int jj = t2 ? j : 0;
  const int hq = 2 * g + jj / 14, rh = (jj % 14) / 7, cb7 = jj % 7;
  const bool t3a = (g < 7) && t2;
  const int cb = 4 * g + jj / 7, rb = jj % 7;
  const bool t3b = g < 7;
  const int fidx = 32 * g + j;
  const bool t4 = (g == 7) && t2;
  const __amdgpu_buffer_rsrc_t rs = __builtin_amdgcn_make_buffer_rsrc((void*)ws, 0, (int)WS_END, 0x00020000);
#define STAMP(i) do { } while (0)
  unsigned cpf = 0u;
  if (wave < 2) {
    const u64 pc = (u64)__builtin_amdgcn_s_getpc();
    const int o = (wave * 64 + lane) * 128;
    if (o < CODEBYTES) cpf = *(const __attribute__((address_space(1))) unsigned*)(pc + o);
  }
  if (tid < 32) lds[L_FLAG + tid] = 0u;
  __syncthreads();

  const bool b0 = lane & 1, b1 = lane & 2, b3 = lane & 8;
  const int lb4 = (lane >> 4) & 1, lb5 = (lane >> 5) & 1;
  v4u wo_r, wgu_r;
  v2u wd_r[8];
  const u16* wo_p = wo + (size_t)(hq * 256 + 128 * rh + 8 * wave + 4 * (lane & 1) + 2 * lb5 + lb4) * 448 + 64 * cb7 + 8 * ((lane >> 1) & 7);
  const u16* wgu_p = (wave < 8 ? wg : wu) + (size_t)(64 * rb + 8 * (wave & 7) + (lane >> 3)) * 1792 + 64 * cb + 8 * (lane & 7);

  if (wave < 8) {
    if (wave == 0) STAMP(0);
    const u16* kb = kc + ((size_t)g * 8192 + 256 * j + 32 * wave + ((lane >> 2) & 3)) * 256 + 8 * ((lane & 3) + 4 * (lane >> 4));
    v4u K[16];
    unsigned tpf = 0u, tpf2 = 0u;
    if (wave == 2) {
      const char* p;
      if (lane < 32) p = (const char*)(vc + ((size_t)g * 8192 + 256 * j) * 256) + lane * 4096;
      else if (lane < 36) p = (const char*)(wo + (size_t)(hq * 256 + 128 * rh + 32 * (lane - 32)) * 448 + 64 * cb7);
      else if (lane < 40) p = (const char*)((lane & 1 ? wg : wu) + (size_t)(64 * rb + 32 * ((lane >> 1) & 1)) * 1792 + 64 * (t3a ? cb : 0));
      else if (lane < 42) p = (const char*)(wd + (size_t)(8 * (t3b ? fidx : 0) + 4 * (lane & 1)) * 448);
      else p = (const char*)out;
      tpf = *(const unsigned*)p;
      tpf2 = __builtin_amdgcn_raw_buffer_load_b32(rs, 65536 + (lane < 21 ? lane : 20) * 65536 - 64, 0, AUX_SC1);
    }
    while (flg[F_EI] < 8u) __builtin_amdgcn_s_sleep(1);
    CBAR();
#pragma unroll
    for (int i = 0; i < 16; ++i) { K[i] = ldnt(kb + (i >> 1) * 1024 + (i & 1) * 128); CBAR(); }
    if (t2) wo_r = ldnt(wo_p);
    if (t3a) wgu_r = ldnt(wgu_p);
    CBAR();
    if (lane == 0) flg[F_KI0 + wave] = 1u;
    {
      while (flg[F_QP] == 0u) __builtin_amdgcn_s_sleep(1);
      CBAR();
      const unsigned tl = lds[L_TAG] & 0xffu, th = (lds[L_TAG] >> 8) | (lds[L_TAG + 1] << 24);
      __builtin_amdgcn_s_setprio(3);
      v4u qa, qb;
      unsigned spins = 0;
      bool ok = false;
      const int off = (int)(WS_QG + (8 * g + wave) * 2048 + (lane >> 5) * 512 + (lane & 31) * 16);
      __builtin_amdgcn_s_sleep(QDELAY);
      for (;;) {
        CBAR();
        if (!ok) {
          qa = __builtin_amdgcn_raw_buffer_load_b128(rs, off, 0, AUX_SC1);
          qb = __builtin_amdgcn_raw_buffer_load_b128(rs, off + 1024, 0, AUX_SC1);
          ok = ((qa[0] & 0xffu) == tl) & (qa[1] == th) & ((qa[2] & 0xffu) == tl) & (qa[3] == th) &
               ((qb[0] & 0xffu) == tl) & (qb[1] == th) & ((qb[2] & 0xffu) == tl) & (qb[3] == th);
        }
        if (__all(ok) || ++spins > SPIN_CAP) break;
        __builtin_amdgcn_s_sleep(8);
      }
      const float c0 = asf(qa[0] & 0xffffff00u) + asf(qb[0] & 0xffffff00u), c1 = asf(qa[2] & 0xffffff00u) + asf(qb[2] & 0xffffff00u);
      const float s0 = tr32(c0, c0), s1 = tr32(c1, c1);
      if (lane < 32) lds[L_Q + 32 * wave + lane] = f2bf(s0) | (f2bf(s1) << 16);
      LGKM0();
      if (lane == 0) LDS_INC(F_QD);
      __builtin_amdgcn_s_setprio(0);
      if (wave == 0) STAMP(2);
    }
    while (flg[F_QD] < 8u) __builtin_amdgcn_s_sleep(1);
    CBAR();
    v4u qf[2];
    {
      const int head = (lane >> 2) & 1;
      const LAS v4u* qp = (const LAS v4u*)(lds + L_Q + head * 128 + 4 * ((lane & 3) + 4 * (lane >> 4)));
      qf[0] = qp[0]; qf[1] = qp[16];
    }
    const int mykg = (lane & 3) | (((lane >> 3) & 1) << 2);
    float s = 0.f;
#pragma unroll
    for (int kg = 0; kg < 8; ++kg) {
      f32x4 acc = {0.f, 0.f, 0.f, 0.f};
      acc = mfma(K[2 * kg], qf[0], acc);
      acc = mfma(K[2 * kg + 1], qf[1], acc);
      float v = b1 ? (b0 ? acc[3] : acc[2]) : (b0 ? acc[1] : acc[0]);
      v += dpp<XOR1>(v); v += dpp<XOR2>(v);
      s = (mykg == kg) ? v : s;
    }
    s = rbf(s) * 0.0625f;
    float m = s;
    m = fmaxf(m, dpp<XOR1>(m)); m = fmaxf(m, dpp<XOR2>(m)); m = fmaxf(m, dpp<ROR8>(m)); m = mx16(m); m = mx32(m);
    if ((lane & ~4) == 0) ldf[L_MW + wave * 2 + (lane >> 2)] = m;
    LGKM0();
    if (lane == 0) LDS_INC(F_MC);
    while (flg[F_MC] < 8u) __builtin_amdgcn_s_sleep(1);
    CBAR();
    {
      const int head = (lane >> 2) & 1;
      float mm = ldf[L_MW + head];
#pragma unroll
      for (int w = 1; w < 8; ++w) mm = fmaxf(mm, ldf[L_MW + 2 * w + head]);
      m = mm;
    }
    const float p = __builtin_amdgcn_exp2f((s - m) * 1.4426950408889634f);
    const unsigned pb = f2bf(p);
    lds[L_P + wave * 64 + ((lane >> 2) & 1) * 32 + (lane >> 4) * 8 + mykg] = pb | (pb << 16);
    float l = asf(pb << 16);
    l += dpp<XOR1>(l); l += dpp<XOR2>(l); l += dpp<ROR8>(l); l = tr16(l, l); l = tr32(l, l);
    if ((lane & ~4) == 0) ldf[L_LW + wave * 2 + (lane >> 2)] = l;
    LGKM0();
    unsigned old = 0;
    if (lane == 0) { flg[F_P0 + wave] = 1u; old = LDS_INC(F_LC); }
    old = __builtin_amdgcn_readfirstlane(old);
    if (old == 7u) {
      while (flg[F_T] == 0u) __builtin_amdgcn_s_sleep(1);
      CBAR();
      __builtin_amdgcn_s_setprio(3);
      if ((lane & ~4) == 0) {
        const int head = lane >> 2;
        float ls = 0.f;
#pragma unroll
        for (int w = 0; w < 8; ++w) ls += ldf[L_LW + 2 * w + head];
        v2u rec = {enc(m), enc(ls)};
        __builtin_amdgcn_raw_buffer_store_b64(rec, rs, (int)(WS_ML + ((2 * g + head) * 32 + j) * 8), 0, AUX_SC1);
      }
      __builtin_amdgcn_s_setprio(0);
    }
    if (wave == 0) STAMP(4);
    asm volatile("" :: "v"(cpf));
    asm volatile("" :: "v"(tpf), "v"(tpf2));
    if (t3a) {
      float a[8];
  #pragma unroll
      for (int c = 0; c < 4; ++c) { a[2 * c] = bflo(wgu_r[c]); a[2 * c + 1] = bfhi(wgu_r[c]); }
      const float z = red8_b345(a, b3);
      ldf[L_CSP + wave * 64 + 8 * (lane & 7) + 4 * lb5 + 2 * lb4 + (lane >> 3 & 1)] = z;
      LGKM0();
      if (lane == 0) LDS_INC(F_CSC);
      if (wave == 4 || wave == 5) {
        while (flg[F_CSC] < 16u || flg[F_T] == 0u) __builtin_amdgcn_s_sleep(4);
        CBAR();
        float cs_ = 0.f;
  #pragma unroll
        for (int w = 0; w < 8; ++w) cs_ += ldf[L_CSP + ((wave - 4) * 8 + w) * 64 + lane];
        __builtin_amdgcn_raw_buffer_store_b32(enc(cs_), rs, (int)(WS_CS + (cb * 7 + rb) * 512 + (wave - 4) * 256 + lane * 4), 0, AUX_SC1);
      }
    }

    if (wave == 7) {
      while (flg[F_T] == 0u) __builtin_amdgcn_s_sleep(2);
      CBAR();
      __builtin_amdgcn_s_setprio(3);
      const unsigned t0 = lds[L_TAG], t1 = lds[L_TAG + 1];
      unsigned spins = 0;
      for (;;) {
        CBAR();
        const v4u r0 = __builtin_amdgcn_raw_buffer_load_b128(rs, (int)(WS_READY + lane * 16), 0, AUX_SC1);
        const v4u r1 = __builtin_amdgcn_raw_buffer_load_b128(rs, (int)(WS_READY + 1024 + lane * 16), 0, AUX_SC1);
        const bool ok = (r0[0] == t0) & (r0[1] == t1) & (r0[2] == t0) & (r0[3] == t1) & (r1[0] == t0) & (r1[1] == t1) & (r1[2] == t0) & (r1[3] == t1);
        if (__all(ok) || ++spins > SPIN_CAP) break;
        __builtin_amdgcn_s_sleep(8);
      }
      if (lane == 0) {
        flg[F_R] = 1u;
        if (blockIdx.x == 0) __builtin_amdgcn_raw_buffer_store_b64(v2u{t0, t1}, rs, (int)WS_E, 0, AUX_SC1);
      }
      if (t2) {
        v2u rec = {1u, 1u};
        bool ok = lane >= 32;
        spins = 0;
        for (;;) {
          CBAR();
          if (!ok) { rec = __builtin_amdgcn_raw_buffer_load_b64(rs, (int)(WS_ML + (hq * 32 + lane) * 8), 0, AUX_SC1); ok = (rec[0] != 0u) & (rec[1] != 0u); }
          if (__all(ok) || ++spins > SPIN_CAP) break;
          __builtin_amdgcn_s_sleep(8);
        }
        const float ms = lane < 32 ? dec(rec[0]) : -INFINITY, lsum = lane < 32 ? dec(rec[1]) : 0.f;
        const float M = wave_max(ms);
        const float e = lane < 32 ? __builtin_amdgcn_exp2f((ms - M) * 1.4426950408889634f) : 0.f;
        const float L = wave_sum(e * lsum);
        if (lane < 32) ldf[L_WTS + lane] = e / L;
        LGKM0();
        if (lane == 0) flg[F_W2] = 1u;
      }
      __builtin_amdgcn_s_setprio(0);
      STAMP(5);
    }
    if (wave == 6 && t3b) {
      while (flg[F_R] == 0u) __builtin_amdgcn_s_sleep(4);
      const int o0 = (int)(WS_CS + ((fidx >> 3) * 7 + (lane >> 4)) * 512 + (((lane >> 3) & 1) * 64 + 8 * (fidx & 7) + (lane & 7)) * 4);
      unsigned c0 = 0u, c1 = lane < 48 ? 0u : 0x80000000u;
      unsigned spins = 0;
      for (;;) {
        CBAR();
        if (c0 == 0u) c0 = __builtin_amdgcn_raw_buffer_load_b32(rs, o0, 0, AUX_SC1);
        if (c1 == 0u) c1 = __builtin_amdgcn_raw_buffer_load_b32(rs, o0 + 2048, 0, AUX_SC1);
        if (__all((c0 != 0u) & (c1 != 0u)) || ++spins > SPIN_CAP) break;
        __builtin_amdgcn_s_sleep(16);
      }
      float cs_ = dec(c0) + dec(c1);
      cs_ = tr16(cs_, cs_); cs_ = tr32(cs_, cs_);
      if (lane < 16) ldf[L_CST + lane] = cs_;
      LGKM0();
      if (lane == 0) flg[F_CS] = 1u;
    }
  } else {
    const int v = wave - 8;
    __builtin_amdgcn_s_setprio(3);
    const v2u ev = __builtin_amdgcn_raw_buffer_load_b64(rs, (int)WS_E, 0, AUX_SC1);
    if (v < 7) {
      const int r0 = 112 * (j & 3) + 16 * v + (lane >> 3);
      const v4u xv = *(const v4u*)(x + 8 * (lane < 56 ? lane : 55));
      const unsigned xr0 = (unsigned)x[r0], xr1 = (unsigned)x[r0 + 8];
      const u16* wqp = wq + (size_t)r0 * 4096 + 64 * (8 * g + (j >> 2)) + 8 * (lane & 7);
      const v4u W0 = ldnt(wqp), W1 = ldnt(wqp + (size_t)8 * 4096);
      CBAR();
      if (lane == 0) LDS_INC(F_EI);
      CBAR();
      float xf[8];
#pragma unroll
      for (int i = 0; i < 4; ++i) { xf[2 * i] = bflo(xv[i]); xf[2 * i + 1] = bfhi(xv[i]); }
      float sm = ((xf[0] + xf[1]) + (xf[2] + xf[3])) + ((xf[4] + xf[5]) + (xf[6] + xf[7]));
      sm = lane < 56 ? sm : 0.f;
      const float mean = wave_sum(sm) * (1.f / 448.f);
      float vs = 0.f;
#pragma unroll
      for (int i = 0; i < 8; ++i) { const float d = xf[i] - mean; vs += d * d; }
      vs = lane < 56 ? vs : 0.f;
      const float var = wave_sum(vs) * (1.f / 448.f);
      const float mb = rbf(mean), den = rbf(__builtin_sqrtf(rbf(rbf(var) + 1e-5f)));
      const float xn0 = rbf(rbf(bflo(xr0) - mb) / den), xn1 = rbf(rbf(bflo(xr1) - mb) / den);
      float acc[8];
#pragma unroll
      for (int c = 0; c < 4; ++c) {
        acc[2 * c] = fmaf(xn1, bflo(W1[c]), xn0 * bflo(W0[c]));
        acc[2 * c + 1] = fmaf(xn1, bfhi(W1[c]), xn0 * bfhi(W0[c]));
      }
      const float qv = red8_b345(acc, b3);
      ldf[L_QP + v * 64 + 8 * (lane & 7) + 4 * lb5 + 2 * lb4 + (lane >> 3 & 1)] = qv;
      LGKM0();
      unsigned oldq = 0;
      if (lane == 0) oldq = LDS_INC(F_QC);
      oldq = __builtin_amdgcn_readfirstlane(oldq);
      if (oldq == 6u) {
        CBAR();
        const u64 T = (((u64)ev[1] << 32) | ev[0]) + 1ull;
        float qs = 0.f;
#pragma unroll
        for (int w = 0; w < 7; ++w) qs += ldf[L_QP + w * 64 + lane];
        const unsigned qbits = (asu(qs) + 0x80u) & 0xffffff00u;
        v2u gr = {qbits | ((unsigned)T & 0xffu), (unsigned)(T >> 8)};
        __builtin_amdgcn_raw_buffer_store_b64(gr, rs, (int)(WS_QG + (8 * g + (j >> 2)) * 2048 + (j & 3) * 512 + lane * 8), 0, AUX_SC1);
        if (lane == 0) { lds[L_TAG] = (unsigned)T; lds[L_TAG + 1] = (unsigned)(T >> 32); LGKM0(); flg[F_QP] = 1u; }
        STAMP(1);
      }
    } else {
      const v4u z4 = {0u, 0u, 0u, 0u};
      __builtin_amdgcn_raw_buffer_store_b128(z4, rs, (int)(WS_PART + ((2 * g) * 32 + j) * 1024 + 16 * lane), 0, AUX_SC1);
      __builtin_amdgcn_raw_buffer_store_b128(z4, rs, (int)(WS_PART + ((2 * g + 1) * 32 + j) * 1024 + 16 * lane), 0, AUX_SC1);
      if (lane < 2) __builtin_amdgcn_raw_buffer_store_b64(v2u{0u, 0u}, rs, (int)(WS_ML + ((2 * g + lane) * 32 + j) * 8), 0, AUX_SC1);
      if (t2 && lane < 16) __builtin_amdgcn_raw_buffer_store_b128(z4, rs, (int)(WS_Y + (cb7 * 32 + hq * 2 + rh) * 256 + 16 * lane), 0, AUX_SC1);
      if (t3a) {
        if (lane < 32) __builtin_amdgcn_raw_buffer_store_b128(z4, rs, (int)(WS_CS + (cb * 7 + rb) * 512 + 16 * lane), 0, AUX_SC1);
        __builtin_amdgcn_raw_buffer_store_b128(z4, rs, (int)(WS_G + (cb * 7 + rb) * 1024 + 16 * lane), 0, AUX_SC1);
      }
      if (t3b) {
        __builtin_amdgcn_raw_buffer_store_b128(z4, rs, (int)(WS_F + fidx * 1792 + 16 * lane), 0, AUX_SC1);
        if (lane < 48) __builtin_amdgcn_raw_buffer_store_b128(z4, rs, (int)(WS_F + fidx * 1792 + 1024 + 16 * lane), 0, AUX_SC1);
      }
      CBAR();
      if (lane == 0) LDS_INC(F_EI);
      VM0();
      const u64 T = (((u64)ev[1] << 32) | ev[0]) + 1ull;
      if (lane == 0) {
        __builtin_amdgcn_raw_buffer_store_b64(v2u{(unsigned)T, (unsigned)(T >> 32)}, rs, (int)(WS_READY + blockIdx.x * 8), 0, AUX_SC1);
        flg[F_T] = 1u;
      }
      STAMP(3);
    }
    __builtin_amdgcn_s_setprio(0);
    const u16* vb = vc + ((size_t)g * 8192 + 256 * j + 32 * v + (lane >> 4)) * 256 + 8 * (lane & 15);
    v4u V[16];
    while (flg[F_KI0 + v] == 0u) __builtin_amdgcn_s_sleep(1);
    CBAR();
    if (t2) wo_r = ldnt(wo_p);
    if (t3a) wgu_r = ldnt(wgu_p);
    if (t3b && v < 2) {
      const int tc = (tid - 512) < 112 ? (tid - 512) : 111;
#pragma unroll
      for (int r = 0; r < 8; ++r) wd_r[r] = __builtin_nontemporal_load((const v2u*)(wd + (size_t)(8 * fidx + r) * 448 + 4 * tc));
    }
    CBAR();
#pragma unroll
    for (int i = 0; i < 16; ++i) { V[i] = ldnt(vb + (i >> 1) * 1024 + (i & 1) * 128); CBAR(); }
    __builtin_amdgcn_s_setprio(2);
    if (t3a) {
      float a[8];
#pragma unroll
      for (int c = 0; c < 4; ++c) { a[2 * c] = bflo(wgu_r[c]); a[2 * c + 1] = bfhi(wgu_r[c]); }
      const float z = red8_b345(a, b3);
      ldf[L_CSP + wave * 64 + 8 * (lane & 7) + 4 * lb5 + 2 * lb4 + (lane >> 3 & 1)] = z;
      LGKM0();
      if (lane == 0) LDS_INC(F_CSC);
    }
    while (flg[F_P0 + v] == 0u) __builtin_amdgcn_s_sleep(2);
    CBAR();
    const LAS v4u* pp = (const LAS v4u*)(lds + L_P + v * 64 + ((lane >> 3) & 1) * 32 + (lane >> 4) * 8);
    const v4u pa = pp[0], pb = pp[1];
    const unsigned hm = (lane & 1) ? 0xffff0000u : 0x0000ffffu;
    const int rsel = (lane >> 1) & 3;
    const unsigned m0 = rsel == 0 ? hm : 0u, m1 = rsel == 1 ? hm : 0u, m2 = rsel == 2 ? hm : 0u, m3 = rsel == 3 ? hm : 0u;
    f32x4 a0 = {0.f, 0.f, 0.f, 0.f}, a1 = {0.f, 0.f, 0.f, 0.f};
#pragma unroll
    for (int kg = 0; kg < 8; ++kg) {
      const unsigned pd = kg < 4 ? pa[kg & 3] : pb[kg & 3];
      const v4u A = {pd & m0, pd & m1, pd & m2, pd & m3};
      a0 = mfma(A, V[2 * kg], a0);
      a1 = mfma(A, V[2 * kg + 1], a1);
    }
    LAS f32x4* rp = (LAS f32x4*)(ldf + L_RED + v * 512 + (lane >> 5) * 256 + 8 * (lane & 15) + 4 * ((lane >> 4) & 1));
    rp[0] = a0; rp[32] = a1;
    LGKM0();
    unsigned oldv = 0;
    if (lane == 0) oldv = LDS_INC(F_VC);
    oldv = __builtin_amdgcn_readfirstlane(oldv);
    if (oldv == 7u) {
      CBAR();
      STAMP(6);
      const LAS f32x4* sp = (const LAS f32x4*)(ldf + L_RED + 4 * lane);
      f32x4 r0[8], r1[8];
#pragma unroll
      for (int w = 0; w < 8; ++w) { r0[w] = sp[w * 128]; r1[w] = sp[w * 128 + 64]; }
      const f32x4 s0 = ((r0[0] + r0[1]) + (r0[2] + r0[3])) + ((r0[4] + r0[5]) + (r0[6] + r0[7]));
      const f32x4 s1 = ((r1[0] + r1[1]) + (r1[2] + r1[3])) + ((r1[4] + r1[5]) + (r1[6] + r1[7]));
      v4u e0 = {enc(s0[0]), enc(s0[1]), enc(s0[2]), enc(s0[3])};
      v4u e1 = {enc(s1[0]), enc(s1[1]), enc(s1[2]), enc(s1[3])};
      __builtin_amdgcn_raw_buffer_store_b128(e0, rs, (int)(WS_PART + ((2 * g) * 32 + j) * 1024 + 16 * lane), 0, AUX_SC1);
      __builtin_amdgcn_raw_buffer_store_b128(e1, rs, (int)(WS_PART + ((2 * g + 1) * 32 + j) * 1024 + 16 * lane), 0, AUX_SC1);
      STAMP(7);
    }
  }
  if (t2) {
    {
      while (flg[F_VC] < 8u || flg[F_R] == 0u) __builtin_amdgcn_s_sleep(1);
      CBAR();
      v4u pa;
      unsigned spins = 0;
      const int off = (int)(WS_PART + (hq * 32 + (lane >> 1)) * 1024 + (128 * rh + 8 * wave + 4 * (lane & 1)) * 4);
      bool ok = false;
      __builtin_amdgcn_s_sleep(T2DELAY);
      for (;;) {
        CBAR();
        if (!ok) {
          pa = __builtin_amdgcn_raw_buffer_load_b128(rs, off, 0, AUX_SC1);
          ok = (pa[0] != 0u) & (pa[1] != 0u) & (pa[2] != 0u) & (pa[3] != 0u);
        }
        if (__all(ok) || ++spins > SPIN_CAP) break;
        __builtin_amdgcn_s_sleep(PSLEEP);
      }
      if (wave == 0) STAMP(8);
      if (wave == 15) STAMP(21);
      if (wave == 7) STAMP(22);
      while (flg[F_W2] == 0u) __builtin_amdgcn_s_sleep(1);
      CBAR();
      const float w = ldf[L_WTS + (lane >> 1)];
      const float u0 = tr32(w * dec(pa[0]), w * dec(pa[2])), u1 = tr32(w * dec(pa[1]), w * dec(pa[3]));
      float o = tr16(u0, u1);
      o += dpp<XOR2>(o); o += dpp<ROR8>(o); o += dpp<ROR4>(o);
      o = rbf(o);
      float y[8];
#pragma unroll
      for (int c = 0; c < 4; ++c) { y[2 * c] = o * bflo(wo_r[c]); y[2 * c + 1] = o * bfhi(wo_r[c]); }
#pragma unroll
      for (int c = 0; c < 4; ++c) y[c] = tr32(y[c], y[c + 4]);
#pragma unroll
      for (int c = 0; c < 2; ++c) y[c] = tr16(y[c], y[c + 2]);
      const float yy = trd<XOR1>(y[0], y[1], b0);
      ldf[L_RED2 + wave * 64 + 8 * ((lane >> 1) & 7) + 4 * lb5 + 2 * lb4 + (lane & 1)] = yy;
    }
    __syncthreads();
    if (wave == 0) {
      STAMP(18);
      const LAS f32x4* rp = (const LAS f32x4*)(ldf + L_RED2 + (lane >> 4) * 256 + 4 * (lane & 15));
      const f32x4 a = (rp[0] + rp[16]) + (rp[32] + rp[48]);
      const float u0 = tr32(a[0], a[2]), u1 = tr32(a[1], a[3]);
      const float z = tr16(u0, u1);
      __builtin_amdgcn_raw_buffer_store_b32(enc(z), rs, (int)(WS_Y + (cb7 * 32 + hq * 2 + rh) * 256 + (4 * (lane & 15) + 2 * lb5 + lb4) * 4), 0, AUX_SC1);
      STAMP(9);
    }
  }
  if (t3a) {
    const unsigned xraw = (unsigned)x[64 * rb + 8 * (wave & 7) + (lane >> 3)];
    if (wave >= 8) {
      v4u yv;
      unsigned spins = 0;
      bool ok = false;
      const int off = (int)(WS_Y + (rb * 32 + 4 * (wave - 8) + (lane >> 4)) * 256 + (lane & 15) * 16);
      __builtin_amdgcn_s_sleep(T3DELAY);
      for (;;) {
        CBAR();
        if (!ok) {
          yv = __builtin_amdgcn_raw_buffer_load_b128(rs, off, 0, AUX_SC1);
          ok = (yv[0] != 0u) & (yv[1] != 0u) & (yv[2] != 0u) & (yv[3] != 0u);
        }
        if (__all(ok) || ++spins > SPIN_CAP) break;
        __builtin_amdgcn_s_sleep(PSLEEP);
      }
      if (wave == 8) STAMP(10);
      if (wave == 15) STAMP(19);
      if (wave == 12) STAMP(20);
      const float u0 = tr32(dec(yv[0]), dec(yv[2])), u1 = tr32(dec(yv[1]), dec(yv[3]));
      ldf[L_HP + (wave - 8) * 64 + 4 * (lane & 15) + 2 * lb5 + lb4] = tr16(u0, u1);
    }
    __syncthreads();
    if (wave == 0) STAMP(14);
    {
      const int row = 8 * (wave & 7) + (lane >> 3);
      float t8[8];
#pragma unroll
      for (int w = 0; w < 8; ++w) t8[w] = ldf[L_HP + w * 64 + row];
      const float at = ((t8[0] + t8[1]) + (t8[2] + t8[3])) + ((t8[4] + t8[5]) + (t8[6] + t8[7]));
      const float h = rbf(bflo(xraw) + rbf(at));
      float a[8];
#pragma unroll
      for (int c = 0; c < 4; ++c) { a[2 * c] = h * bflo(wgu_r[c]); a[2 * c + 1] = h * bfhi(wgu_r[c]); }
      const float z = red8_b345(a, b3);
      ldf[L_RED3 + wave * 64 + 8 * (lane & 7) + 4 * lb5 + 2 * lb4 + (lane >> 3 & 1)] = z;
      if (wave < 8 && (lane & 7) == 0) ldf[L_QP + row] = h;
    }
    __syncthreads();
    if (wave == 0) STAMP(15);
    if (wave < 2) {
      float t8[8];
#pragma unroll
      for (int w = 0; w < 8; ++w) t8[w] = ldf[L_RED3 + (wave * 8 + w) * 64 + lane];
      const float z = ((t8[0] + t8[1]) + (t8[2] + t8[3])) + ((t8[4] + t8[5]) + (t8[6] + t8[7]));
      __builtin_amdgcn_raw_buffer_store_b32(enc(z), rs, (int)(WS_G + (cb * 7 + rb) * 1024 + (lane >> 3) * 128 + (8 * wave + (lane & 7)) * 4), 0, AUX_SC1);
    } else if (wave == 2) {
      const float h = ldf[L_QP + lane];
      const float piv = asf((unsigned)__builtin_amdgcn_readfirstlane((int)asu(h)));
      const float d = h - piv;
      float st = tr32(d, d * d);
      st = tr16(st, st); st += dpp<XOR1>(st); st += dpp<XOR2>(st); st += dpp<ROR8>(st); st += dpp<ROR4>(st);
      const int k = lane >> 3;
      if (k == 0 || k == 1 || k == 4) {
        const float val = k == 0 ? piv : st;
        __builtin_amdgcn_raw_buffer_store_b32(enc(val), rs, (int)(WS_G + (cb * 7 + rb) * 1024 + (lane & 7) * 128 + (16 + (k == 4 ? 2 : k)) * 4), 0, AUX_SC1);
      }
    }
    if (wave == 0) STAMP(16);
  }
  if (t3b) {
    if (wave == 8 || wave == 9) {
      if (!t3a) { while (flg[F_VC] < 8u || flg[F_R] == 0u) __builtin_amdgcn_s_sleep(1); __builtin_amdgcn_s_sleep(40); }
      v4u gv = {1u, 1u, 1u, 1u};
      bool ok = (lane >= 56) || ((lane & 7) > 4);
      unsigned spins = 0;
      const int off = (int)(WS_G + ((fidx >> 3) * 7 + (lane >> 3)) * 1024 + (fidx & 7) * 128 + (lane & 7) * 16);
      __builtin_amdgcn_s_sleep(T3BDELAY);
      for (;;) {
        CBAR();
        if (!ok) {
          gv = __builtin_amdgcn_raw_buffer_load_b128(rs, off, 0, AUX_SC1);
          ok = (gv[0] != 0u) & (gv[1] != 0u) & (gv[2] != 0u) & ((gv[3] != 0u) | ((lane & 7) == 4));
        }
        if (__all(ok) || ++spins > SPIN_CAP) break;
        __builtin_amdgcn_s_sleep(PSLEEP);
      }
      if (wave == 8) STAMP(17);
      while (flg[F_CS] == 0u) __builtin_amdgcn_s_sleep(1);
      CBAR();
      const bool live = lane < 56;
      const bool isst = (lane & 7) == 4;
      const float piv = dec(gv[0]), s1 = dec(gv[1]), s2 = dec(gv[2]);
      const float dm = s1 * (1.f / 64.f);
      const float mw = piv + dm, m2 = s2 - s1 * dm;
      float a0 = live ? (isst ? mw : dec(gv[0])) : 0.f, a1 = live ? dec(gv[1]) : 0.f, a2 = live ? dec(gv[2]) : 0.f, a3 = live ? dec(gv[3]) : 0.f;
      a0 = tr32(a0, a0); a1 = tr32(a1, a1); a2 = tr32(a2, a2); a3 = tr32(a3, a3);
      a0 = tr16(a0, a0); a1 = tr16(a1, a1); a2 = tr16(a2, a2); a3 = tr16(a3, a3);
      a0 += dpp<ROR8>(a0); a1 += dpp<ROR8>(a1); a2 += dpp<ROR8>(a2); a3 += dpp<ROR8>(a3);
      const float mean = asf((unsigned)__builtin_amdgcn_readlane((int)asu(a0), 4)) * (1.f / 7.f);
      const float dd = mw - mean;
      float vv = (live && isst) ? m2 + 64.f * dd * dd : 0.f;
      vv = tr32(vv, vv); vv = tr16(vv, vv); vv += dpp<ROR8>(vv);
      const float var = fmaxf(asf((unsigned)__builtin_amdgcn_readlane((int)asu(vv), 4)) * (1.f / 448.f), 0.f);
      const float mb = rbf(mean), den = rbf(__builtin_sqrtf(rbf(rbf(var) + 1e-5f)));
      const float rden = 1.f / den;
      const f32x4 cs4 = *(const LAS f32x4*)(ldf + L_CST + 4 * (lane & 3));
      const float z0 = rbf((a0 - mb * cs4[0]) * rden), z1 = rbf((a1 - mb * cs4[1]) * rden), z2 = rbf((a2 - mb * cs4[2]) * rden), z3 = rbf((a3 - mb * cs4[3]) * rden);
      const float u0 = dpp<XOR2>(z0), u1 = dpp<XOR2>(z1), u2 = dpp<XOR2>(z2), u3 = dpp<XOR2>(z3);
      float av[4];
      {
        const float zz[4] = {z0, z1, z2, z3}, uu[4] = {u0, u1, u2, u3};
#pragma unroll
        for (int i = 0; i < 4; ++i) {
          const float z = zz[i];
          const float t = 0.7978845608028654f * (z + 0.044715f * z * z * z);
          const float e2 = __builtin_amdgcn_exp2f(t * 2.8853900817779268f);
          const float th = 1.f - 2.f * __builtin_amdgcn_rcpf(e2 + 1.f);
          av[i] = rbf(rbf(0.5f * z * (1.f + th)) * uu[i]);
        }
      }
      f32x4 f = {0.f, 0.f, 0.f, 0.f};
#pragma unroll
      for (int r = 0; r < 8; ++r) {
        const float ar = asf((unsigned)__builtin_amdgcn_readlane((int)asu(av[r & 3]), r >> 2));
        f[0] = fmaf(ar, bflo(wd_r[r][0]), f[0]); f[1] = fmaf(ar, bfhi(wd_r[r][0]), f[1]);
        f[2] = fmaf(ar, bflo(wd_r[r][1]), f[2]); f[3] = fmaf(ar, bfhi(wd_r[r][1]), f[3]);
      }
      if (tid - 512 < 112) {
        v4u e = {enc(f[0]), enc(f[1]), enc(f[2]), enc(f[3])};
        __builtin_amdgcn_raw_buffer_store_b128(e, rs, (int)(WS_F + fidx * 1792 + 16 * (tid - 512)), 0, AUX_SC1);
      }
      if (wave == 8) STAMP(11);
    }
  } else if (t4) {
    const int rid = j;
    {
      const int off = wave < 14 ? (int)(WS_F + (16 * wave + (lane >> 2)) * 1792 + (16 * rid + 4 * (lane & 3)) * 4)
                                : (int)(WS_Y + ((rid >> 2) * 32 + 16 * (wave - 14) + (lane >> 2)) * 256 + (16 * (rid & 3) + 4 * (lane & 3)) * 4);
      v4u pv;
      unsigned spins = 0;
      bool ok = false;
      if (wave < 14) { while (flg[F_Y4] < 2u) __builtin_amdgcn_s_sleep(1); __builtin_amdgcn_s_sleep(T4DELAY); }
      else __builtin_amdgcn_s_sleep(T3DELAY);
      for (;;) {
        CBAR();
        if (!ok) {
          pv = __builtin_amdgcn_raw_buffer_load_b128(rs, off, 0, AUX_SC1);
          ok = (pv[0] != 0u) & (pv[1] != 0u) & (pv[2] != 0u) & (pv[3] != 0u);
        }
        if (__all(ok) || ++spins > SPIN_CAP) break;
        __builtin_amdgcn_s_sleep(PSLEEP);
      }
      if (wave >= 14 && lane == 0) LDS_INC(F_Y4);
      if (wave == 0) STAMP(12);
      const float a0 = tr32(dec(pv[0]), dec(pv[2])), a1 = tr32(dec(pv[1]), dec(pv[3]));
      float z = tr16(a0, a1);
      z += dpp<ROR8>(z); z += dpp<ROR4>(z);
      const int col = 4 * (lane & 3) + 2 * lb5 + lb4;
      if ((lane & 12) == 0) ldf[(wave < 14 ? L_RED4 + wave * 16 : L_H4 + (wave - 14) * 16) + col] = z;
    }
    const unsigned xraw = (unsigned)x[16 * rid + (tid & 15)];
    __syncthreads();
    if (tid < 16) {
      float t[14];
#pragma unroll
      for (int w = 0; w < 14; ++w) t[w] = ldf[L_RED4 + w * 16 + tid];
      const float f = (((t[0] + t[1]) + (t[2] + t[3])) + ((t[4] + t[5]) + (t[6] + t[7]))) + (((t[8] + t[9]) + (t[10] + t[11])) + (t[12] + t[13]));
      const float h = rbf(bflo(xraw) + rbf(ldf[L_H4 + tid] + ldf[L_H4 + 16 + tid]));
      out[16 * rid + tid] = (u16)f2bf(h + rbf(f));
    }
    if (wave == 0) STAMP(13);
  }
}

extern "C" void kernel_launch(void* const* d_in, const int* in_sizes, int n_in,
                              void* d_out, int out_size, void* d_ws, size_t ws_size,
                              hipStream_t stream) {
  fused_decode<<<256, 1024, 0, stream>>>((const u16*)d_in[0], (const u16*)d_in[1], (const u16*)d_in[2], (const u16*)d_in[3],
                                         (const u16*)d_in[4], (const u16*)d_in[6], (const u16*)d_in[7], (const u16*)d_in[8],
                                         (u16*)d_out, (unsigned char*)d_ws);
}
```

```cpp
#define DIAG 31
#include <hip/hip_runtime.h>
#include <stdint.h>

typedef unsigned short u16;
typedef unsigned long long u64;
typedef unsigned v4u __attribute__((ext_vector_type(4)));
typedef unsigned v2u __attribute__((ext_vector_type(2)));
typedef float f32x4 __attribute__((ext_vector_type(4)));
typedef short bf16x8 __attribute__((ext_vector_type(8)));
#define LAS __attribute__((address_space(3)))

#define STAMPS 0

constexpr int AUX_SC1 = 16;
constexpr unsigned WS_E = 0;
constexpr unsigned WS_READY = 4096;
constexpr unsigned WS_QG = 8192;
constexpr unsigned WS_ML = 139264;
constexpr unsigned WS_Y = 143360;
constexpr unsigned WS_CS = 200704;
constexpr unsigned WS_G = 301056;
constexpr unsigned WS_PART = 501760;
constexpr unsigned WS_F = 1026048;
constexpr unsigned WS_END = 1427456;
constexpr unsigned WS_STAMP = 2u << 20;
constexpr unsigned SPIN_CAP = 1u << 17;
#define CODEPF 1
#define CODEBYTES 13824
#define QDELAY 16
#define T2DELAY 8
#define T3DELAY 8
#define T3BDELAY 8
#define T4DELAY 40
#define PSLEEP 4

__device__ __forceinline__ float asf(unsigned u) { return __builtin_bit_cast(float, u); }
__device__ __forceinline__ unsigned asu(float f) { return __builtin_bit_cast(unsigned, f); }
__device__ __forceinline__ float bflo(unsigned w) { return asf(w << 16); }
__device__ __forceinline__ float bfhi(unsigned w) { return asf(w & 0xffff0000u); }
__device__ __forceinline__ unsigned f2bf(float f) { __bf16 b = (__bf16)f; return (unsigned)__builtin_bit_cast(u16, b); }
__device__ __forceinline__ float rbf(float f) { return asf(f2bf(f) << 16); }
__device__ __forceinline__ unsigned enc(float f) { unsigned u = asu(f); u = (u == 0x80000000u) ? 0u : u; return u ^ 0x80000000u; }
__device__ __forceinline__ float dec(unsigned u) { return asf(u ^ 0x80000000u); }

template <int CTRL> __device__ __forceinline__ float dpp(float x) {
  return asf((unsigned)__builtin_amdgcn_mov_dpp((int)asu(x), CTRL, 0xf, 0xf, true));
}
constexpr int XOR1 = 0xB1, XOR2 = 0x4E, ROR4 = 0x124, ROR8 = 0x128;
__device__ __forceinline__ float tr32(float a, float b) {
  auto r = __builtin_amdgcn_permlane32_swap(asu(a), asu(b), false, false);
  return asf(r[0]) + asf(r[1]);
}
__device__ __forceinline__ float tr16(float a, float b) {
  auto r = __builtin_amdgcn_permlane16_swap(asu(a), asu(b), false, false);
  return asf(r[0]) + asf(r[1]);
}
template <int CTRL> __device__ __forceinline__ float trd(float a, float b, bool bit) {
  const float t = bit ? a : b, k = bit ? b : a;
  return k + dpp<CTRL>(t);
}
__device__ __forceinline__ float mx32(float a) {
  auto r = __builtin_amdgcn_permlane32_swap(asu(a), asu(a), false, false);
  return fmaxf(asf(r[0]), asf(r[1]));
}
__device__ __forceinline__ float mx16(float a) {
  auto r = __builtin_amdgcn_permlane16_swap(asu(a), asu(a), false, false);
  return fmaxf(asf(r[0]), asf(r[1]));
}
__device__ __forceinline__ float wave_sum(float x) {
  x += dpp<XOR1>(x); x += dpp<XOR2>(x); x += dpp<ROR8>(x); x += dpp<ROR4>(x);
  x = tr16(x, x); x = tr32(x, x);
  return x;
}
__device__ __forceinline__ float wave_max(float x) {
  x = fmaxf(x, dpp<XOR1>(x)); x = fmaxf(x, dpp<XOR2>(x)); x = fmaxf(x, dpp<ROR8>(x)); x = fmaxf(x, dpp<ROR4>(x));
  x = mx16(x); x = mx32(x);
  return x;
}
__device__ __forceinline__ float red8_b345(float (&a)[8], bool b3) {
#pragma unroll
  for (int c = 0; c < 4; ++c) a[c] = tr32(a[c], a[c + 4]);
#pragma unroll
  for (int c = 0; c < 2; ++c) a[c] = tr16(a[c], a[c + 2]);
  return trd<ROR8>(a[0], a[1], b3);
}
#define KVPOL 1
__device__ __forceinline__ v4u ldnt(const u16* p) {
  return __builtin_nontemporal_load((const v4u*)p);
}
__device__ __forceinline__ f32x4 mfma(v4u a, v4u b, f32x4 c) {
  return __builtin_amdgcn_mfma_f32_16x16x32_bf16(__builtin_bit_cast(bf16x8, a), __builtin_bit_cast(bf16x8, b), c, 0, 0, 0);
}
#define LGKM0() asm volatile("s_waitcnt lgkmcnt(0)" ::: "memory")
#define VM0() asm volatile("s_waitcnt vmcnt(0)" ::: "memory")
#define CBAR() asm volatile("" ::: "memory")
#define LDS_INC(f) __hip_atomic_fetch_add((LAS unsigned*)(lds + L_FLAG + (f)), 1u, __ATOMIC_RELAXED, __HIP_MEMORY_SCOPE_WORKGROUP)

constexpr int L_Q = 0;
constexpr int L_QP = 256;
constexpr int L_P = 704;
constexpr int L_MW = 1216;
constexpr int L_LW = 1232;
constexpr int L_WTS = 1248;
constexpr int L_FLAG = 1280;
constexpr int L_TAG = 1312;
constexpr int L_CST = 1328;
constexpr int L_RED2 = 1344;
constexpr int L_HP = 2368;
constexpr int L_RED3 = 2880;
constexpr int L_CSP = 3904;
constexpr int L_RED4 = 4928;
constexpr int L_H4 = 5152;
constexpr int L_RED = 5184;
constexpr int L_TOTAL = L_RED + 4096;
constexpr int F_QD = 0, F_R = 1, F_MC = 2, F_LC = 3, F_VC = 4, F_T = 5, F_QC = 6, F_EI = 7, F_P0 = 8, F_KI0 = 16,
              F_Y4 = 24, F_W2 = 25, F_QP = 26, F_CSC = 27, F_CS = 28;

__global__ void __launch_bounds__(1024) fused_decode(const u16* __restrict__ x, const u16* __restrict__ kc, const u16* __restrict__ vc,
                                                     const u16* __restrict__ wq, const u16* __restrict__ wo, const u16* __restrict__ wg,
                                                     const u16* __restrict__ wu, const u16* __restrict__ wd, u16* __restrict__ out,
                                                     unsigned char* __restrict__ ws) {
  __shared__ __attribute__((aligned(16))) unsigned lds_raw[L_TOTAL];
  LAS unsigned* lds = (LAS unsigned*)lds_raw;
  LAS float* ldf = (LAS float*)lds_raw;
  volatile LAS unsigned* flg = (volatile LAS unsigned*)(lds + L_FLAG);
  const int tid = threadIdx.x, lane = tid & 63, wave = __builtin_amdgcn_readfirstlane(tid >> 6);
  const int g = blockIdx.x & 7, j = blockIdx.x >> 3;
  const bool t2 = j < 28;
  const int jj = t2 ? j : 0;
  const int hq = 2 * g + jj / 14, rh = (jj % 14) / 7, cb7 = jj % 7;
  const bool t3a = (g < 7) && t2;
  const int cb = 4 * g + jj / 7, rb = jj % 7;
  const bool t3b = g < 7;
  const int fidx = 32 * g + j;
  const bool t4 = (g == 7) && t2;
  const __amdgpu_buffer_rsrc_t rs = __builtin_amdgcn_make_buffer_rsrc((void*)ws, 0, (int)WS_END, 0x00020000);
  __shared__ u64 stl[32];
  const u64 tstart = __builtin_amdgcn_s_memrealtime();
  if (tid < 32) stl[tid] = tstart;
#define STAMP(i) do { if (lane == 0) stl[i] = __builtin_amdgcn_s_memrealtime(); } while (0)
  unsigned cpf = 0u;
  if (wave < 2) {
    const u64 pc = (u64)__builtin_amdgcn_s_getpc();
    const int o = (wave * 64 + lane) * 128;
    if (o < CODEBYTES) cpf = *(const __attribute__((address_space(1))) unsigned*)(pc + o);
  }
  if (tid < 32) lds[L_FLAG + tid] = 0u;
  __syncthreads();

  const bool b0 = lane & 1, b1 = lane & 2, b3 = lane & 8;
  const int lb4 = (lane >> 4) & 1, lb5 = (lane >> 5) & 1;
  v4u wo_r, wgu_r;
  v2u wd_r[8];
  const u16* wo_p = wo + (size_t)(hq * 256 + 128 * rh + 8 * wave + 4 * (lane & 1) + 2 * lb5 + lb4) * 448 + 64 * cb7 + 8 * ((lane >> 1) & 7);
  const u16* wgu_p = (wave < 8 ? wg : wu) + (size_t)(64 * rb + 8 * (wave & 7) + (lane >> 3)) * 1792 + 64 * cb + 8 * (lane & 7);

  if (wave < 8) {
    if (wave == 0) STAMP(0);
    const u16* kb = kc + ((size_t)g * 8192 + 256 * j + 32 * wave + ((lane >> 2) & 3)) * 256 + 8 * ((lane & 3) + 4 * (lane >> 4));
    v4u K[16];
    while (flg[F_EI] < 8u) __builtin_amdgcn_s_sleep(1);
    CBAR();
#pragma unroll
    for (int i = 0; i < 16; ++i) { K[i] = ldnt(kb + (i >> 1) * 1024 + (i & 1) * 128); CBAR(); }
    if (t2) wo_r = ldnt(wo_p);
    if (t3a) wgu_r = ldnt(wgu_p);
    CBAR();
    if (lane == 0) flg[F_KI0 + wave] = 1u;
    {
      while (flg[F_QP] == 0u) __builtin_amdgcn_s_sleep(1);
      CBAR();
      const unsigned tl = lds[L_TAG] & 0xffu, th = (lds[L_TAG] >> 8) | (lds[L_TAG + 1] << 24);
      __builtin_amdgcn_s_setprio(3);
      v4u qa, qb;
      unsigned spins = 0;
      bool ok = false;
      const int off = (int)(WS_QG + (8 * g + wave) * 2048 + (lane >> 5) * 512 + (lane & 31) * 16);
      __builtin_amdgcn_s_sleep(QDELAY);
      for (;;) {
        CBAR();
        if (!ok) {
          qa = __builtin_amdgcn_raw_buffer_load_b128(rs, off, 0, AUX_SC1);
          qb = __builtin_amdgcn_raw_buffer_load_b128(rs, off + 1024, 0, AUX_SC1);
          ok = ((qa[0] & 0xffu) == tl) & (qa[1] == th) & ((qa[2] & 0xffu) == tl) & (qa[3] == th) &
               ((qb[0] & 0xffu) == tl) & (qb[1] == th) & ((qb[2] & 0xffu) == tl) & (qb[3] == th);
        }
        if (__all(ok) || ++spins > SPIN_CAP) break;
        __builtin_amdgcn_s_sleep(8);
      }
      const float c0 = asf(qa[0] & 0xffffff00u) + asf(qb[0] & 0xffffff00u), c1 = asf(qa[2] & 0xffffff00u) + asf(qb[2] & 0xffffff00u);
      const float s0 = tr32(c0, c0), s1 = tr32(c1, c1);
      if (lane < 32) lds[L_Q + 32 * wave + lane] = f2bf(s0) | (f2bf(s1) << 16);
      LGKM0();
      if (lane == 0) LDS_INC(F_QD);
      __builtin_amdgcn_s_setprio(0);
      if (wave == 0) STAMP(2);
    }
    while (flg[F_QD] < 8u) __builtin_amdgcn_s_sleep(1);
    CBAR();
    v4u qf[2];
    {
      const int head = (lane >> 2) & 1;
      const LAS v4u* qp = (const LAS v4u*)(lds + L_Q + head * 128 + 4 * ((lane & 3) + 4 * (lane >> 4)));
      qf[0] = qp[0]; qf[1] = qp[16];
    }
    const int mykg = (lane & 3) | (((lane >> 3) & 1) << 2);
    float s = 0.f;
#pragma unroll
    for (int kg = 0; kg < 8; ++kg) {
      f32x4 acc = {0.f, 0.f, 0.f, 0.f};
      acc = mfma(K[2 * kg], qf[0], acc);
      acc = mfma(K[2 * kg + 1], qf[1], acc);
      float v = b1 ? (b0 ? acc[3] : acc[2]) : (b0 ? acc[1] : acc[0]);
      v += dpp<XOR1>(v); v += dpp<XOR2>(v);
      s = (mykg == kg) ? v : s;
    }
    s = rbf(s) * 0.0625f;
    float m = s;
    m = fmaxf(m, dpp<XOR1>(m)); m = fmaxf(m, dpp<XOR2>(m)); m = fmaxf(m, dpp<ROR8>(m)); m = mx16(m); m = mx32(m);
    if ((lane & ~4) == 0) ldf[L_MW + wave * 2 + (lane >> 2)] = m;
    LGKM0();
    if (lane == 0) LDS_INC(F_MC);
    while (flg[F_MC] < 8u) __builtin_amdgcn_s_sleep(1);
    CBAR();
    {
      const int head = (lane >> 2) & 1;
      float mm = ldf[L_MW + head];
#pragma unroll
      for (int w = 1; w < 8; ++w) mm = fmaxf(mm, ldf[L_MW + 2 * w + head]);
      m = mm;
    }
    const float p = __builtin_amdgcn_exp2f((s - m) * 1.4426950408889634f);
    const unsigned pb = f2bf(p);
    lds[L_P + wave * 64 + ((lane >> 2) & 1) * 32 + (lane >> 4) * 8 + mykg] = pb | (pb << 16);
    float l = asf(pb << 16);
    l += dpp<XOR1>(l); l += dpp<XOR2>(l); l += dpp<ROR8>(l); l = tr16(l, l); l = tr32(l, l);
    if ((lane & ~4) == 0) ldf[L_LW + wave * 2 + (lane >> 2)] = l;
    LGKM0();
    unsigned old = 0;
    if (lane == 0) { flg[F_P0 + wave] = 1u; old = LDS_INC(F_LC); }
    old = __builtin_amdgcn_readfirstlane(old);
    if (old == 7u) {
      while (flg[F_T] == 0u) __builtin_amdgcn_s_sleep(1);
      CBAR();
      __builtin_amdgcn_s_setprio(3);
      if ((lane & ~4) == 0) {
        const int head = lane >> 2;
        float ls = 0.f;
#pragma unroll
        for (int w = 0; w < 8; ++w) ls += ldf[L_LW + 2 * w + head];
        v2u rec = {enc(m), enc(ls)};
        __builtin_amdgcn_raw_buffer_store_b64(rec, rs, (int)(WS_ML + ((2 * g + head) * 32 + j) * 8), 0, AUX_SC1);
      }
      __builtin_amdgcn_s_setprio(0);
    }
    if (wave == 0) STAMP(4);
    asm volatile("" :: "v"(cpf));
    if (t3a) {
      float a[8];
  #pragma unroll
      for (int c = 0; c < 4; ++c) { a[2 * c] = bflo(wgu_r[c]); a[2 * c + 1] = bfhi(wgu_r[c]); }
      const float z = red8_b345(a, b3);
      ldf[L_CSP + wave * 64 + 8 * (lane & 7) + 4 * lb5 + 2 * lb4 + (lane >> 3 & 1)] = z;
      LGKM0();
      if (lane == 0) LDS_INC(F_CSC);
      if (wave == 4 || wave == 5) {
        while (flg[F_CSC] < 16u || flg[F_T] == 0u) __builtin_amdgcn_s_sleep(4);
        CBAR();
        float cs_ = 0.f;
  #pragma unroll
        for (int w = 0; w < 8; ++w) cs_ += ldf[L_CSP + ((wave - 4) * 8 + w) * 64 + lane];
        __builtin_amdgcn_raw_buffer_store_b32(enc(cs_), rs, (int)(WS_CS + (cb * 7 + rb) * 512 + (wave - 4) * 256 + lane * 4), 0, AUX_SC1);
      }
    }

    if (wave == 7) {
      while (flg[F_T] == 0u) __builtin_amdgcn_s_sleep(2);
      CBAR();
      __builtin_amdgcn_s_setprio(3);
      const unsigned t0 = lds[L_TAG], t1 = lds[L_TAG + 1];
      unsigned spins = 0;
      for (;;) {
        CBAR();
        const v4u r0 = __builtin_amdgcn_raw_buffer_load_b128(rs, (int)(WS_READY + lane * 16), 0, AUX_SC1);
        const v4u r1 = __builtin_amdgcn_raw_buffer_load_b128(rs, (int)(WS_READY + 1024 + lane * 16), 0, AUX_SC1);
        const bool ok = (r0[0] == t0) & (r0[1] == t1) & (r0[2] == t0) & (r0[3] == t1) & (r1[0] == t0) & (r1[1] == t1) & (r1[2] == t0) & (r1[3] == t1);
        if (__all(ok) || ++spins > SPIN_CAP) break;
        __builtin_amdgcn_s_sleep(8);
      }
      if (lane == 0) {
        flg[F_R] = 1u;
        if (blockIdx.x == 0) __builtin_amdgcn_raw_buffer_store_b64(v2u{t0, t1}, rs, (int)WS_E, 0, AUX_SC1);
      }
      if (t2) {
        v2u rec = {1u, 1u};
        bool ok = lane >= 32;
        spins = 0;
        for (;;) {
          CBAR();
          if (!ok) { rec = __builtin_amdgcn_raw_buffer_load_b64(rs, (int)(WS_ML + (hq * 32 + lane) * 8), 0, AUX_SC1); ok = (rec[0] != 0u) & (rec[1] != 0u); }
          if (__all(ok) || ++spins > SPIN_CAP) break;
          __builtin_amdgcn_s_sleep(8);
        }
        const float ms = lane < 32 ? dec(rec[0]) : -INFINITY, lsum = lane < 32 ? dec(rec[1]) : 0.f;
        const float M = wave_max(ms);
        const float e = lane < 32 ? __builtin_amdgcn_exp2f((ms - M) * 1.4426950408889634f) : 0.f;
        const float L = wave_sum(e * lsum);
        if (lane < 32) ldf[L_WTS + lane] = e / L;
        LGKM0();
        if (lane == 0) flg[F_W2] = 1u;
      }
      __builtin_amdgcn_s_setprio(0);
      STAMP(5);
    }
    if (wave == 6 && t3b) {
      while (flg[F_R] == 0u) __builtin_amdgcn_s_sleep(4);
      const int o0 = (int)(WS_CS + ((fidx >> 3) * 7 + (lane >> 4)) * 512 + (((lane >> 3) & 1) * 64 + 8 * (fidx & 7) + (lane & 7)) * 4);
      unsigned c0 = 0u, c1 = lane < 48 ? 0u : 0x80000000u;
      unsigned spins = 0;
      for (;;) {
        CBAR();
        if (c0 == 0u) c0 = __builtin_amdgcn_raw_buffer_load_b32(rs, o0, 0, AUX_SC1);
        if (c1 == 0u) c1 = __builtin_amdgcn_raw_buffer_load_b32(rs, o0 + 2048, 0, AUX_SC1);
        if (__all((c0 != 0u) & (c1 != 0u)) || ++spins > SPIN_CAP) break;
        __builtin_amdgcn_s_sleep(16);
      }
      float cs_ = dec(c0) + dec(c1);
      cs_ = tr16(cs_, cs_); cs_ = tr32(cs_, cs_);
      if (lane < 16) ldf[L_CST + lane] = cs_;
      LGKM0();
      if (lane == 0) flg[F_CS] = 1u;
    }
  } else {
    const int v = wave - 8;
    __builtin_amdgcn_s_setprio(3);
    const v2u ev = __builtin_amdgcn_raw_buffer_load_b64(rs, (int)WS_E, 0, AUX_SC1);
    if (v < 7) {
      const int r0 = 112 * (j & 3) + 16 * v + (lane >> 3);
      const v4u xv = *(const v4u*)(x + 8 * (lane < 56 ? lane : 55));
      const unsigned xr0 = (unsigned)x[r0], xr1 = (unsigned)x[r0 + 8];
      const u16* wqp = wq + (size_t)r0 * 4096 + 64 * (8 * g + (j >> 2)) + 8 * (lane & 7);
      const v4u W0 = ldnt(wqp), W1 = ldnt(wqp + (size_t)8 * 4096);
      CBAR();
      if (lane == 0) LDS_INC(F_EI);
      CBAR();
      float xf[8];
#pragma unroll
      for (int i = 0; i < 4; ++i) { xf[2 * i] = bflo(xv[i]); xf[2 * i + 1] = bfhi(xv[i]); }
      float sm = ((xf[0] + xf[1]) + (xf[2] + xf[3])) + ((xf[4] + xf[5]) + (xf[6] + xf[7]));
      sm = lane < 56 ? sm : 0.f;
      const float mean = wave_sum(sm) * (1.f / 448.f);
      float vs = 0.f;
#pragma unroll
      for (int i = 0; i < 8; ++i) { const float d = xf[i] - mean; vs += d * d; }
      vs = lane < 56 ? vs : 0.f;
      const float var = wave_sum(vs) * (1.f / 448.f);
      const float mb = rbf(mean), den = rbf(__builtin_sqrtf(rbf(rbf(var) + 1e-5f)));
      const float xn0 = rbf(rbf(bflo(xr0) - mb) / den), xn1 = rbf(rbf(bflo(xr1) - mb) / den);
      float acc[8];
#pragma unroll
      for (int c = 0; c < 4; ++c) {
        acc[2 * c] = fmaf(xn1, bflo(W1[c]), xn0 * bflo(W0[c]));
        acc[2 * c + 1] = fmaf(xn1, bfhi(W1[c]), xn0 * bfhi(W0[c]));
      }
      const float qv = red8_b345(acc, b3);
      ldf[L_QP + v * 64 + 8 * (lane & 7) + 4 * lb5 + 2 * lb4 + (lane >> 3 & 1)] = qv;
      LGKM0();
      unsigned oldq = 0;
      if (lane == 0) oldq = LDS_INC(F_QC);
      oldq = __builtin_amdgcn_readfirstlane(oldq);
      if (oldq == 6u) {
        CBAR();
        const u64 T = (((u64)ev[1] << 32) | ev[0]) + 1ull;
        float qs = 0.f;
#pragma unroll
        for (int w = 0; w < 7; ++w) qs += ldf[L_QP + w * 64 + lane];
        const unsigned qbits = (asu(qs) + 0x80u) & 0xffffff00u;
        v2u gr = {qbits | ((unsigned)T & 0xffu), (unsigned)(T >> 8)};
        __builtin_amdgcn_raw_buffer_store_b64(gr, rs, (int)(WS_QG + (8 * g + (j >> 2)) * 2048 + (j & 3) * 512 + lane * 8), 0, AUX_SC1);
        if (lane == 0) { lds[L_TAG] = (unsigned)T; lds[L_TAG + 1] = (unsigned)(T >> 32); LGKM0(); flg[F_QP] = 1u; }
        STAMP(1);
      }
    } else {
      const v4u z4 = {0u, 0u, 0u, 0u};
      __builtin_amdgcn_raw_buffer_store_b128(z4, rs, (int)(WS_PART + ((2 * g) * 32 + j) * 1024 + 16 * lane), 0, AUX_SC1);
      __builtin_amdgcn_raw_buffer_store_b128(z4, rs, (int)(WS_PART + ((2 * g + 1) * 32 + j) * 1024 + 16 * lane), 0, AUX_SC1);
      if (lane < 2) __builtin_amdgcn_raw_buffer_store_b64(v2u{0u, 0u}, rs, (int)(WS_ML + ((2 * g + lane) * 32 + j) * 8), 0, AUX_SC1);
      if (t2 && lane < 16) __builtin_amdgcn_raw_buffer_store_b128(z4, rs, (int)(WS_Y + (cb7 * 32 + hq * 2 + rh) * 256 + 16 * lane), 0, AUX_SC1);
      if (t3a) {
        if (lane < 32) __builtin_amdgcn_raw_buffer_store_b128(z4, rs, (int)(WS_CS + (cb * 7 + rb) * 512 + 16 * lane), 0, AUX_SC1);
        __builtin_amdgcn_raw_buffer_store_b128(z4, rs, (int)(WS_G + (cb * 7 + rb) * 1024 + 16 * lane), 0, AUX_SC1);
      }
      if (t3b) {
        __builtin_amdgcn_raw_buffer_store_b128(z4, rs, (int)(WS_F + fidx * 1792 + 16 * lane), 0, AUX_SC1);
        if (lane < 48) __builtin_amdgcn_raw_buffer_store_b128(z4, rs, (int)(WS_F + fidx * 1792 + 1024 + 16 * lane), 0, AUX_SC1);
      }
      CBAR();
      if (lane == 0) LDS_INC(F_EI);
      VM0();
      const u64 T = (((u64)ev[1] << 32) | ev[0]) + 1ull;
      if (lane == 0) {
        __builtin_amdgcn_raw_buffer_store_b64(v2u{(unsigned)T, (unsigned)(T >> 32)}, rs, (int)(WS_READY + blockIdx.x * 8), 0, AUX_SC1);
        flg[F_T] = 1u;
      }
      STAMP(3);
    }
    __builtin_amdgcn_s_setprio(0);
    const u16* vb = vc + ((size_t)g * 8192 + 256 * j + 32 * v + (lane >> 4)) * 256 + 8 * (lane & 15);
    v4u V[16];
    while (flg[F_KI0 + v] == 0u) __builtin_amdgcn_s_sleep(1);
    CBAR();
    if (t2) wo_r = ldnt(wo_p);
    if (t3a) wgu_r = ldnt(wgu_p);
    if (t3b && v < 2) {
      const int tc = (tid - 512) < 112 ? (tid - 512) : 111;
#pragma unroll
      for (int r = 0; r < 8; ++r) wd_r[r] = __builtin_nontemporal_load((const v2u*)(wd + (size_t)(8 * fidx + r) * 448 + 4 * tc));
    }
    CBAR();
#pragma unroll
    for (int i = 0; i < 16; ++i) { V[i] = ldnt(vb + (i >> 1) * 1024 + (i & 1) * 128); CBAR(); }
    __builtin_amdgcn_s_setprio(2);
    if (t3a) {
      float a[8];
#pragma unroll
      for (int c = 0; c < 4; ++c) { a[2 * c] = bflo(wgu_r[c]); a[2 * c + 1] = bfhi(wgu_r[c]); }
      const float z = red8_b345(a, b3);
      ldf[L_CSP + wave * 64 + 8 * (lane & 7) + 4 * lb5 + 2 * lb4 + (lane >> 3 & 1)] = z;
      LGKM0();
      if (lane == 0) LDS_INC(F_CSC);
    }
    while (flg[F_P0 + v] == 0u) __builtin_amdgcn_s_sleep(2);
    CBAR();
    const LAS v4u* pp = (const LAS v4u*)(lds + L_P + v * 64 + ((lane >> 3) & 1) * 32 + (lane >> 4) * 8);
    const v4u pa = pp[0], pb = pp[1];
    const unsigned hm = (lane & 1) ? 0xffff0000u : 0x0000ffffu;
    const int rsel = (lane >> 1) & 3;
    const unsigned m0 = rsel == 0 ? hm : 0u, m1 = rsel == 1 ? hm : 0u, m2 = rsel == 2 ? hm : 0u, m3 = rsel == 3 ? hm : 0u;
    f32x4 a0 = {0.f, 0.f, 0.f, 0.f}, a1 = {0.f, 0.f, 0.f, 0.f};
#pragma unroll
    for (int kg = 0; kg < 8; ++kg) {
      const unsigned pd = kg < 4 ? pa[kg & 3] : pb[kg & 3];
      const v4u A = {pd & m0, pd & m1, pd & m2, pd & m3};
      a0 = mfma(A, V[2 * kg], a0);
      a1 = mfma(A, V[2 * kg + 1], a1);
    }
    LAS f32x4* rp = (LAS f32x4*)(ldf + L_RED + v * 512 + (lane >> 5) * 256 + 8 * (lane & 15) + 4 * ((lane >> 4) & 1));
    rp[0] = a0; rp[32] = a1;
    LGKM0();
    unsigned oldv = 0;
    if (lane == 0) oldv = LDS_INC(F_VC);
    oldv = __builtin_amdgcn_readfirstlane(oldv);
    if (oldv == 7u) {
      CBAR();
      STAMP(6);
      const LAS f32x4* sp = (const LAS f32x4*)(ldf + L_RED + 4 * lane);
      f32x4 r0[8], r1[8];
#pragma unroll
      for (int w = 0; w < 8; ++w) { r0[w] = sp[w * 128]; r1[w] = sp[w * 128 + 64]; }
      const f32x4 s0 = ((r0[0] + r0[1]) + (r0[2] + r0[3])) + ((r0[4] + r0[5]) + (r0[6] + r0[7]));
      const f32x4 s1 = ((r1[0] + r1[1]) + (r1[2] + r1[3])) + ((r1[4] + r1[5]) + (r1[6] + r1[7]));
      v4u e0 = {enc(s0[0]), enc(s0[1]), enc(s0[2]), enc(s0[3])};
      v4u e1 = {enc(s1[0]), enc(s1[1]), enc(s1[2]), enc(s1[3])};
      __builtin_amdgcn_raw_buffer_store_b128(e0, rs, (int)(WS_PART + ((2 * g) * 32 + j) * 1024 + 16 * lane), 0, AUX_SC1);
      __builtin_amdgcn_raw_buffer_store_b128(e1, rs, (int)(WS_PART + ((2 * g + 1) * 32 + j) * 1024 + 16 * lane), 0, AUX_SC1);
      STAMP(7);
    }
  }
  if (t2) {
    {
      while (flg[F_VC] < 8u || flg[F_R] == 0u) __builtin_amdgcn_s_sleep(1);
      CBAR();
      v4u pa;
      unsigned spins = 0;
      const int off = (int)(WS_PART + (hq * 32 + (lane >> 1)) * 1024 + (128 * rh + 8 * wave + 4 * (lane & 1)) * 4);
      bool ok = false;
      __builtin_amdgcn_s_sleep(T2DELAY);
      for (;;) {
        CBAR();
        if (!ok) {
          pa = __builtin_amdgcn_raw_buffer_load_b128(rs, off, 0, AUX_SC1);
          ok = (pa[0] != 0u) & (pa[1] != 0u) & (pa[2] != 0u) & (pa[3] != 0u);
        }
        if (__all(ok) || ++spins > SPIN_CAP) break;
        __builtin_amdgcn_s_sleep(PSLEEP);
      }
      if (wave == 0) STAMP(8);
      if (wave == 15) STAMP(21);
      if (wave == 7) STAMP(22);
      while (flg[F_W2] == 0u) __builtin_amdgcn_s_sleep(1);
      CBAR();
      const float w = ldf[L_WTS + (lane >> 1)];
      const float u0 = tr32(w * dec(pa[0]), w * dec(pa[2])), u1 = tr32(w * dec(pa[1]), w * dec(pa[3]));
      float o = tr16(u0, u1);
      o += dpp<XOR2>(o); o += dpp<ROR8>(o); o += dpp<ROR4>(o);
      o = rbf(o);
      float y[8];
#pragma unroll
      for (int c = 0; c < 4; ++c) { y[2 * c] = o * bflo(wo_r[c]); y[2 * c + 1] = o * bfhi(wo_r[c]); }
#pragma unroll
      for (int c = 0; c < 4; ++c) y[c] = tr32(y[c], y[c + 4]);
#pragma unroll
      for (int c = 0; c < 2; ++c) y[c] = tr16(y[c], y[c + 2]);
      const float yy = trd<XOR1>(y[0], y[1], b0);
      ldf[L_RED2 + wave * 64 + 8 * ((lane >> 1) & 7) + 4 * lb5 + 2 * lb4 + (lane & 1)] = yy;
    }
    __syncthreads();
    if (wave == 0) {
      STAMP(18);
      const LAS f32x4* rp = (const LAS f32x4*)(ldf + L_RED2 + (lane >> 4) * 256 + 4 * (lane & 15));
      const f32x4 a = (rp[0] + rp[16]) + (rp[32] + rp[48]);
      const float u0 = tr32(a[0], a[2]), u1 = tr32(a[1], a[3]);
      const float z = tr16(u0, u1);
      __builtin_amdgcn_raw_buffer_store_b32(enc(z), rs, (int)(WS_Y + (cb7 * 32 + hq * 2 + rh) * 256 + (4 * (lane & 15) + 2 * lb5 + lb4) * 4), 0, AUX_SC1);
      STAMP(9);
    }
  }
  if (t3a) {
    const unsigned xraw = (unsigned)x[64 * rb + 8 * (wave & 7) + (lane >> 3)];
    if (wave >= 8) {
      v4u yv;
      unsigned spins = 0;
      bool ok = false;
      const int off = (int)(WS_Y + (rb * 32 + 4 * (wave - 8) + (lane >> 4)) * 256 + (lane & 15) * 16);
      __builtin_amdgcn_s_sleep(T3DELAY);
      for (;;) {
        CBAR();
        if (!ok) {
          yv = __builtin_amdgcn_raw_buffer_load_b128(rs, off, 0, AUX_SC1);
          ok = (yv[0] != 0u) & (yv[1] != 0u) & (yv[2] != 0u) & (yv[3] != 0u);
        }
        if (__all(ok) || ++spins > SPIN_CAP) break;
        __builtin_amdgcn_s_sleep(PSLEEP);
      }
      if (wave == 8) STAMP(10);
      if (wave == 15) STAMP(19);
      if (wave == 12) STAMP(20);
      const float u0 = tr32(dec(yv[0]), dec(yv[2])), u1 = tr32(dec(yv[1]), dec(yv[3]));
      ldf[L_HP + (wave - 8) * 64 + 4 * (lane & 15) + 2 * lb5 + lb4] = tr16(u0, u1);
    }
    __syncthreads();
    if (wave == 0) STAMP(14);
    {
      const int row = 8 * (wave & 7) + (lane >> 3);
      float t8[8];
#pragma unroll
      for (int w = 0; w < 8; ++w) t8[w] = ldf[L_HP + w * 64 + row];
      const float at = ((t8[0] + t8[1]) + (t8[2] + t8[3])) + ((t8[4] + t8[5]) + (t8[6] + t8[7]));
      const float h = rbf(bflo(xraw) + rbf(at));
      float a[8];
#pragma unroll
      for (int c = 0; c < 4; ++c) { a[2 * c] = h * bflo(wgu_r[c]); a[2 * c + 1] = h * bfhi(wgu_r[c]); }
      const float z = red8_b345(a, b3);
      ldf[L_RED3 + wave * 64 + 8 * (lane & 7) + 4 * lb5 + 2 * lb4 + (lane >> 3 & 1)] = z;
      if (wave < 8 && (lane & 7) == 0) ldf[L_QP + row] = h;
    }
    __syncthreads();
    if (wave == 0) STAMP(15);
    if (wave < 2) {
      float t8[8];
#pragma unroll
      for (int w = 0; w < 8; ++w) t8[w] = ldf[L_RED3 + (wave * 8 + w) * 64 + lane];
      const float z = ((t8[0] + t8[1]) + (t8[2] + t8[3])) + ((t8[4] + t8[5]) + (t8[6] + t8[7]));
      __builtin_amdgcn_raw_buffer_store_b32(enc(z), rs, (int)(WS_G + (cb * 7 + rb) * 1024 + (lane >> 3) * 128 + (8 * wave + (lane & 7)) * 4), 0, AUX_SC1);
    } else if (wave == 2) {
      const float h = ldf[L_QP + lane];
      const float piv = asf((unsigned)__builtin_amdgcn_readfirstlane((int)asu(h)));
      const float d = h - piv;
      float st = tr32(d, d * d);
      st = tr16(st, st); st += dpp<XOR1>(st); st += dpp<XOR2>(st); st += dpp<ROR8>(st); st += dpp<ROR4>(st);
      const int k = lane >> 3;
      if (k == 0 || k == 1 || k == 4) {
        const float val = k == 0 ? piv : st;
        __builtin_amdgcn_raw_buffer_store_b32(enc(val), rs, (int)(WS_G + (cb * 7 + rb) * 1024 + (lane & 7) * 128 + (16 + (k == 4 ? 2 : k)) * 4), 0, AUX_SC1);
      }
    }
    if (wave == 0) STAMP(16);
  }
  if (t3b) {
    if (wave == 8 || wave == 9) {
      if (!t3a) { while (flg[F_VC] < 8u || flg[F_R] == 0u) __builtin_amdgcn_s_sleep(1); __builtin_amdgcn_s_sleep(40); }
      v4u gv = {1u, 1u, 1u, 1u};
      bool ok = (lane >= 56) || ((lane & 7) > 4);
      unsigned spins = 0;
      const int off = (int)(WS_G + ((fidx >> 3) * 7 + (lane >> 3)) * 1024 + (fidx & 7) * 128 + (lane & 7) * 16);
      __builtin_amdgcn_s_sleep(T3BDELAY);
      for (;;) {
        CBAR();
        if (!ok) {
          gv = __builtin_amdgcn_raw_buffer_load_b128(rs, off, 0, AUX_SC1);
          ok = (gv[0] != 0u) & (gv[1] != 0u) & (gv[2] != 0u) & ((gv[3] != 0u) | ((lane & 7) == 4));
        }
        if (__all(ok) || ++spins > SPIN_CAP) break;
        __builtin_amdgcn_s_sleep(PSLEEP);
      }
      if (wave == 8) STAMP(17);
      while (flg[F_CS] == 0u) __builtin_amdgcn_s_sleep(1);
      CBAR();
      const bool live = lane < 56;
      const bool isst = (lane & 7) == 4;
      const float piv = dec(gv[0]), s1 = dec(gv[1]), s2 = dec(gv[2]);
      const float dm = s1 * (1.f / 64.f);
      const float mw = piv + dm, m2 = s2 - s1 * dm;
      float a0 = live ? (isst ? mw : dec(gv[0])) : 0.f, a1 = live ? dec(gv[1]) : 0.f, a2 = live ? dec(gv[2]) : 0.f, a3 = live ? dec(gv[3]) : 0.f;
      a0 = tr32(a0, a0); a1 = tr32(a1, a1); a2 = tr32(a2, a2); a3 = tr32(a3, a3);
      a0 = tr16(a0, a0); a1 = tr16(a1, a1); a2 = tr16(a2, a2); a3 = tr16(a3, a3);
      a0 += dpp<ROR8>(a0); a1 += dpp<ROR8>(a1); a2 += dpp<ROR8>(a2); a3 += dpp<ROR8>(a3);
      const float mean = asf((unsigned)__builtin_amdgcn_readlane((int)asu(a0), 4)) * (1.f / 7.f);
      const float dd = mw - mean;
      float vv = (live && isst) ? m2 + 64.f * dd * dd : 0.f;
      vv = tr32(vv, vv); vv = tr16(vv, vv); vv += dpp<ROR8>(vv);
      const float var = fmaxf(asf((unsigned)__builtin_amdgcn_readlane((int)asu(vv), 4)) * (1.f / 448.f), 0.f);
      const float mb = rbf(mean), den = rbf(__builtin_sqrtf(rbf(rbf(var) + 1e-5f)));
      const float rden = 1.f / den;
      const f32x4 cs4 = *(const LAS f32x4*)(ldf + L_CST + 4 * (lane & 3));
      const float z0 = rbf((a0 - mb * cs4[0]) * rden), z1 = rbf((a1 - mb * cs4[1]) * rden), z2 = rbf((a2 - mb * cs4[2]) * rden), z3 = rbf((a3 - mb * cs4[3]) * rden);
      const float u0 = dpp<XOR2>(z0), u1 = dpp<XOR2>(z1), u2 = dpp<XOR2>(z2), u3 = dpp<XOR2>(z3);
      float av[4];
      {
        const float zz[4] = {z0, z1, z2, z3}, uu[4] = {u0, u1, u2, u3};
#pragma unroll
        for (int i = 0; i < 4; ++i) {
          const float z = zz[i];
          const float t = 0.7978845608028654f * (z + 0.044715f * z * z * z);
          const float e2 = __builtin_amdgcn_exp2f(t * 2.8853900817779268f);
          const float th = 1.f - 2.f * __builtin_amdgcn_rcpf(e2 + 1.f);
          av[i] = rbf(rbf(0.5f * z * (1.f + th)) * uu[i]);
        }
      }
      f32x4 f = {0.f, 0.f, 0.f, 0.f};
#pragma unroll
      for (int r = 0; r < 8; ++r) {
        const float ar = asf((unsigned)__builtin_amdgcn_readlane((int)asu(av[r & 3]), r >> 2));
        f[0] = fmaf(ar, bflo(wd_r[r][0]), f[0]); f[1] = fmaf(ar, bfhi(wd_r[r][0]), f[1]);
        f[2] = fmaf(ar, bflo(wd_r[r][1]), f[2]); f[3] = fmaf(ar, bfhi(wd_r[r][1]), f[3]);
      }
      if (tid - 512 < 112) {
        v4u e = {enc(f[0]), enc(f[1]), enc(f[2]), enc(f[3])};
        __builtin_amdgcn_raw_buffer_store_b128(e, rs, (int)(WS_F + fidx * 1792 + 16 * (tid - 512)), 0, AUX_SC1);
      }
      if (wave == 8) STAMP(11);
    }
  } else if (t4) {
    const int rid = j;
    {
      const int off = wave < 14 ? (int)(WS_F + (16 * wave + (lane >> 2)) * 1792 + (16 * rid + 4 * (lane & 3)) * 4)
                                : (int)(WS_Y + ((rid >> 2) * 32 + 16 * (wave - 14) + (lane >> 2)) * 256 + (16 * (rid & 3) + 4 * (lane & 3)) * 4);
      v4u pv;
      unsigned spins = 0;
      bool ok = false;
      if (wave < 14) { while (flg[F_Y4] < 2u) __builtin_amdgcn_s_sleep(1); __builtin_amdgcn_s_sleep(T4DELAY); }
      else __builtin_amdgcn_s_sleep(T3DELAY);
      for (;;) {
        CBAR();
        if (!ok) {
          pv = __builtin_amdgcn_raw_buffer_load_b128(rs, off, 0, AUX_SC1);
          ok = (pv[0] != 0u) & (pv[1] != 0u) & (pv[2] != 0u) & (pv[3] != 0u);
        }
        if (__all(ok) || ++spins > SPIN_CAP) break;
        __builtin_amdgcn_s_sleep(PSLEEP);
      }
      if (wave >= 14 && lane == 0) LDS_INC(F_Y4);
      if (wave == 0) STAMP(12);
      const float a0 = tr32(dec(pv[0]), dec(pv[2])), a1 = tr32(dec(pv[1]), dec(pv[3]));
      float z = tr16(a0, a1);
      z += dpp<ROR8>(z); z += dpp<ROR4>(z);
      const int col = 4 * (lane & 3) + 2 * lb5 + lb4;
      if ((lane & 12) == 0) ldf[(wave < 14 ? L_RED4 + wave * 16 : L_H4 + (wave - 14) * 16) + col] = z;
    }
    const unsigned xraw = (unsigned)x[16 * rid + (tid & 15)];
    __syncthreads();
    if (tid < 16) {
      float t[14];
#pragma unroll
      for (int w = 0; w < 14; ++w) t[w] = ldf[L_RED4 + w * 16 + tid];
      const float f = (((t[0] + t[1]) + (t[2] + t[3])) + ((t[4] + t[5]) + (t[6] + t[7]))) + (((t[8] + t[9]) + (t[10] + t[11])) + (t[12] + t[13]));
      const float h = rbf(bflo(xraw) + rbf(ldf[L_H4 + tid] + ldf[L_H4 + 16 + tid]));
      out[16 * rid + tid] = (u16)f2bf(h + rbf(f));
    }
    if (wave == 0) STAMP(13);
  }
  __syncthreads();
  if (wave == 0) {
    const u64 d = stl[DIAG] - tstart;
    while (__builtin_amdgcn_s_memrealtime() - tstart < 5000ull + d) __builtin_amdgcn_s_sleep(1);
  }
}

extern "C" void kernel_launch(void* const* d_in, const int* in_sizes, int n_in,
                              void* d_out, int out_size, void* d_ws, size_t ws_size,
                              hipStream_t stream) {
  fused_decode<<<256, 1024, 0, stream>>>((const u16*)d_in[0], (const u16*)d_in[1], (const u16*)d_in[2], (const u16*)d_in[3],
                                         (const u16*)d_in[4], (const u16*)d_in[6], (const u16*)d_in[7], (const u16*)d_in[8],
                                         (u16*)d_out, (unsigned char*)d_ws);
}
```

```cpp
#include <hip/hip_runtime.h>
#include <stdint.h>

typedef unsigned short u16;
typedef unsigned long long u64;
typedef unsigned v4u __attribute__((ext_vector_type(4)));
typedef unsigned v2u __attribute__((ext_vector_type(2)));
typedef float f32x4 __attribute__((ext_vector_type(4)));
typedef short bf16x8 __attribute__((ext_vector_type(8)));
#define LAS __attribute__((address_space(3)))

#define STAMPS 0

constexpr int AUX_SC1 = 16;
constexpr unsigned WS_E = 0;
constexpr unsigned WS_READY = 4096;
constexpr unsigned WS_QG = 8192;
constexpr unsigned WS_ML = 139264;
constexpr unsigned WS_Y = 143360;
constexpr unsigned WS_CS = 200704;
constexpr unsigned WS_G = 301056;
constexpr unsigned WS_PART = 501760;
constexpr unsigned WS_F = 1026048;
constexpr unsigned WS_END = 1427456;
constexpr unsigned WS_STAMP = 2u << 20;
constexpr unsigned SPIN_CAP = 1u << 17;
#define CODEPF 1
#define CODEBYTES 13824
#define QDELAY 16
#define T2DELAY 8
#define T3DELAY 8
#define T3BDELAY 8
#define T4DELAY 40
#define PSLEEP 4

__device__ __forceinline__ float asf(unsigned u) { return __builtin_bit_cast(float, u); }
__device__ __forceinline__ unsigned asu(float f) { return __builtin_bit_cast(unsigned, f); }
__device__ __forceinline__ float bflo(unsigned w) { return asf(w << 16); }
__device__ __forceinline__ float bfhi(unsigned w) { return asf(w & 0xffff0000u); }
__device__ __forceinline__ unsigned f2bf(float f) { __bf16 b = (__bf16)f; return (unsigned)__builtin_bit_cast(u16, b); }
__device__ __forceinline__ float rbf(float f) { return asf(f2bf(f) << 16); }
__device__ __forceinline__ unsigned enc(float f) { unsigned u = asu(f); u = (u == 0x80000000u) ? 0u : u; return u ^ 0x80000000u; }
__device__ __forceinline__ float dec(unsigned u) { return asf(u ^ 0x80000000u); }

template <int CTRL> __device__ __forceinline__ float dpp(float x) {
  return asf((unsigned)__builtin_amdgcn_mov_dpp((int)asu(x), CTRL, 0xf, 0xf, true));
}
constexpr int XOR1 = 0xB1, XOR2 = 0x4E, ROR4 = 0x124, ROR8 = 0x128;
__device__ __forceinline__ float tr32(float a, float b) {
  auto r = __builtin_amdgcn_permlane32_swap(asu(a), asu(b), false, false);
  return asf(r[0]) + asf(r[1]);
}
__device__ __forceinline__ float tr16(float a, float b) {
  auto r = __builtin_amdgcn_permlane16_swap(asu(a), asu(b), false, false);
  return asf(r[0]) + asf(r[1]);
}
template <int CTRL> __device__ __forceinline__ float trd(float a, float b, bool bit) {
  const float t = bit ? a : b, k = bit ? b : a;
  return k + dpp<CTRL>(t);
}
__device__ __forceinline__ float mx32(float a) {
  auto r = __builtin_amdgcn_permlane32_swap(asu(a), asu(a), false, false);
  return fmaxf(asf(r[0]), asf(r[1]));
}
__device__ __forceinline__ float mx16(float a) {
  auto r = __builtin_amdgcn_permlane16_swap(asu(a), asu(a), false, false);
  return fmaxf(asf(r[0]), asf(r[1]));
}
__device__ __forceinline__ float wave_sum(float x) {
  x += dpp<XOR1>(x); x += dpp<XOR2>(x); x += dpp<ROR8>(x); x += dpp<ROR4>(x);
  x = tr16(x, x); x = tr32(x, x);
  return x;
}
__device__ __forceinline__ float wave_max(float x) {
  x = fmaxf(x, dpp<XOR1>(x)); x = fmaxf(x, dpp<XOR2>(x)); x = fmaxf(x, dpp<ROR8>(x)); x = fmaxf(x, dpp<ROR4>(x));
  x = mx16(x); x = mx32(x);
  return x;
}
__device__ __forceinline__ float red8_b345(float (&a)[8], bool b3) {
#pragma unroll
  for (int c = 0; c < 4; ++c) a[c] = tr32(a[c], a[c + 4]);
#pragma unroll
  for (int c = 0; c < 2; ++c) a[c] = tr16(a[c], a[c + 2]);
  return trd<ROR8>(a[0], a[1], b3);
}
#define KVPOL 1
__device__ __forceinline__ v4u ldnt(const u16* p) {
  return __builtin_nontemporal_load((const v4u*)p);
}
__device__ __forceinline__ f32x4 mfma(v4u a, v4u b, f32x4 c) {
  return __builtin_amdgcn_mfma_f32_16x16x32_bf16(__builtin_bit_cast(bf16x8, a), __builtin_bit_cast(bf16x8, b), c, 0, 0, 0);
}
#define LGKM0() asm volatile("s_waitcnt lgkmcnt(0)" ::: "memory")
#define VM0() asm volatile("s_waitcnt vmcnt(0)" ::: "memory")
#define CBAR() asm volatile("" ::: "memory")
#define LDS_INC(f) __hip_atomic_fetch_add((LAS unsigned*)(lds + L_FLAG + (f)), 1u, __ATOMIC_RELAXED, __HIP_MEMORY_SCOPE_WORKGROUP)

constexpr int L_Q = 0;
constexpr int L_QP = 256;
constexpr int L_P = 704;
constexpr int L_MW = 1216;
constexpr int L_LW = 1232;
constexpr int L_WTS = 1248;
constexpr int L_FLAG = 1280;
constexpr int L_TAG = 1312;
constexpr int L_CST = 1328;
constexpr int L_RED2 = 1344;
constexpr int L_HP = 2368;
constexpr int L_RED3 = 2880;
constexpr int L_CSP = 3904;
constexpr int L_RED4 = 4928;
constexpr int L_H4 = 5152;
constexpr int L_RED = 5184;
constexpr int L_TOTAL = L_RED + 4096;
constexpr int F_QD = 0, F_R = 1, F_MC = 2, F_LC = 3, F_VC = 4, F_T = 5, F_QC = 6, F_EI = 7, F_P0 = 8, F_KI0 = 16,
              F_Y4 = 24, F_W2 = 25, F_QP = 26, F_CSC = 27, F_CS = 28;

__global__ void __launch_bounds__(1024) fused_decode(const u16* __restrict__ x, const u16* __restrict__ kc, const u16* __restrict__ vc,
                                                     const u16* __restrict__ wq, const u16* __restrict__ wo, const u16* __restrict__ wg,
                                                     const u16* __restrict__ wu, const u16* __restrict__ wd, u16* __restrict__ out,
                                                     unsigned char* __restrict__ ws) {
  __shared__ __attribute__((aligned(16))) unsigned lds_raw[L_TOTAL];
  LAS unsigned* lds = (LAS unsigned*)lds_raw;
  LAS float* ldf = (LAS float*)lds_raw;
  volatile LAS unsigned* flg = (volatile LAS unsigned*)(lds + L_FLAG);
  const int tid = threadIdx.x, lane = tid & 63, wave = __builtin_amdgcn_readfirstlane(tid >> 6);
  const int g = blockIdx.x & 7, j = blockIdx.x >> 3;
  const bool t2 = j < 28;
  const int jj = t2 ? j : 0;
  const int hq = 2 * g + jj / 14, rh = (jj % 14) / 7, cb7 = jj % 7;
  const bool t3a = (g < 7) && t2;
  const int cb = 4 * g + jj / 7, rb = jj % 7;
  const bool t3b = g < 7;
  const int fidx = 32 * g + j;
  const bool t4 = (g == 7) && t2;
  const __amdgpu_buffer_rsrc_t rs = __builtin_amdgcn_make_buffer_rsrc((void*)ws, 0, (int)WS_END, 0x00020000);
#define STAMP(i) do { } while (0)
  unsigned cpf = 0u;
  if (wave < 2) {
    const u64 pc = (u64)__builtin_amdgcn_s_getpc();
    const int o = (wave * 64 + lane) * 128;
    if (o < CODEBYTES) cpf = *(const __attribute__((address_space(1))) unsigned*)(pc + o);
  }
  if (tid < 32) lds[L_FLAG + tid] = 0u;
  __syncthreads();

  const bool b0 = lane & 1, b1 = lane & 2, b3 = lane & 8;
  const int lb4 = (lane >> 4) & 1, lb5 = (lane >> 5) & 1;
  v4u wo_r, wgu_r;
  v2u wd_r[8];
  const u16* wo_p = wo + (size_t)(hq * 256 + 128 * rh + 8 * wave + 4 * (lane & 1) + 2 * lb5 + lb4) * 448 + 64 * cb7 + 8 * ((lane >> 1) & 7);
  const u16* wgu_p = (wave < 8 ? wg : wu) + (size_t)(64 * rb + 8 * (wave & 7) + (lane >> 3)) * 1792 + 64 * cb + 8 * (lane & 7);

  if (wave < 8) {
    if (wave == 0) STAMP(0);
    const u16* kb = kc + ((size_t)g * 8192 + 256 * j + 32 * wave + ((lane >> 2) & 3)) * 256 + 8 * ((lane & 3) + 4 * (lane >> 4));
    v4u K[16];
    while (flg[F_EI] < 8u) __builtin_amdgcn_s_sleep(1);
    CBAR();
#pragma unroll
    for (int i = 0; i < 16; ++i) { K[i] = ldnt(kb + (i >> 1) * 1024 + (i & 1) * 128); CBAR(); }
    if (t2) wo_r = ldnt(wo_p);
    if (t3a) wgu_r = ldnt(wgu_p);
    CBAR();
    if (lane == 0) flg[F_KI0 + wave] = 1u;
    {
      while (flg[F_QP] == 0u) __builtin_amdgcn_s_sleep(1);
      CBAR();
      const unsigned tl = lds[L_TAG] & 0xffu, th = (lds[L_TAG] >> 8) | (lds[L_TAG + 1] << 24);
      __builtin_amdgcn_s_setprio(3);
      v4u qa, qb;
      unsigned spins = 0;
      bool ok = false;
      const int off = (int)(WS_QG + (8 * g + wave) * 2048 + (lane >> 5) * 512 + (lane & 31) * 16);
      __builtin_amdgcn_s_sleep(QDELAY);
      for (;;) {
        CBAR();
        if (!ok) {
          qa = __builtin_amdgcn_raw_buffer_load_b128(rs, off, 0, AUX_SC1);
          qb = __builtin_amdgcn_raw_buffer_load_b128(rs, off + 1024, 0, AUX_SC1);
          ok = ((qa[0] & 0xffu) == tl) & (qa[1] == th) & ((qa[2] & 0xffu) == tl) & (qa[3] == th) &
               ((qb[0] & 0xffu) == tl) & (qb[1] == th) & ((qb[2] & 0xffu) == tl) & (qb[3] == th);
        }
        if (__all(ok) || ++spins > SPIN_CAP) break;
        __builtin_amdgcn_s_sleep(8);
      }
      const float c0 = asf(qa[0] & 0xffffff00u) + asf(qb[0] & 0xffffff00u), c1 = asf(qa[2] & 0xffffff00u) + asf(qb[2] & 0xffffff00u);
      const float s0 = tr32(c0, c0), s1 = tr32(c1, c1);
      if (lane < 32) lds[L_Q + 32 * wave + lane] = f2bf(s0) | (f2bf(s1) << 16);
      LGKM0();
      if (lane == 0) LDS_INC(F_QD);
      __builtin_amdgcn_s_setprio(0);
      if (wave == 0) STAMP(2);
    }
    while (flg[F_QD] < 8u) __builtin_amdgcn_s_sleep(1);
    CBAR();
    v4u qf[2];
    {
      const int head = (lane >> 2) & 1;
      const LAS v4u* qp = (const LAS v4u*)(lds + L_Q + head * 128 + 4 * ((lane & 3) + 4 * (lane >> 4)));
      qf[0] = qp[0]; qf[1] = qp[16];
    }
    const int mykg = (lane & 3) | (((lane >> 3) & 1) << 2);
    float s = 0.f;
#pragma unroll
    for (int kg = 0; kg < 8; ++kg) {
      f32x4 acc = {0.f, 0.f, 0.f, 0.f};
      acc = mfma(K[2 * kg], qf[0], acc);
      acc = mfma(K[2 * kg + 1], qf[1], acc);
      float v = b1 ? (b0 ? acc[3] : acc[2]) : (b0 ? acc[1] : acc[0]);
      v += dpp<XOR1>(v); v += dpp<XOR2>(v);
      s = (mykg == kg) ? v : s;
    }
    s = rbf(s) * 0.0625f;
    float m = s;
    m = fmaxf(m, dpp<XOR1>(m)); m = fmaxf(m, dpp<XOR2>(m)); m = fmaxf(m, dpp<ROR8>(m)); m = mx16(m); m = mx32(m);
    if ((lane & ~4) == 0) ldf[L_MW + wave * 2 + (lane >> 2)] = m;
    LGKM0();
    if (lane == 0) LDS_INC(F_MC);
    while (flg[F_MC] < 8u) __builtin_amdgcn_s_sleep(1);
    CBAR();
    {
      const int head = (lane >> 2) & 1;
      float mm = ldf[L_MW + head];
#pragma unroll
      for (int w = 1; w < 8; ++w) mm = fmaxf(mm, ldf[L_MW + 2 * w + head]);
      m = mm;
    }
    const float p = __builtin_amdgcn_exp2f((s - m) * 1.4426950408889634f);
    const unsigned pb = f2bf(p);
    lds[L_P + wave * 64 + ((lane >> 2) & 1) * 32 + (lane >> 4) * 8 + mykg] = pb | (pb << 16);
    float l = asf(pb << 16);
    l += dpp<XOR1>(l); l += dpp<XOR2>(l); l += dpp<ROR8>(l); l = tr16(l, l); l = tr32(l, l);
    if ((lane & ~4) == 0) ldf[L_LW + wave * 2 + (lane >> 2)] = l;
    LGKM0();
    unsigned old = 0;
    if (lane == 0) { flg[F_P0 + wave] = 1u; old = LDS_INC(F_LC); }
    old = __builtin_amdgcn_readfirstlane(old);
    if (old == 7u) {
      while (flg[F_T] == 0u) __builtin_amdgcn_s_sleep(1);
      CBAR();
      __builtin_amdgcn_s_setprio(3);
      if ((lane & ~4) == 0) {
        const int head = lane >> 2;
        float ls = 0.f;
#pragma unroll
        for (int w = 0; w < 8; ++w) ls += ldf[L_LW + 2 * w + head];
        v2u rec = {enc(m), enc(ls)};
        __builtin_amdgcn_raw_buffer_store_b64(rec, rs, (int)(WS_ML + ((2 * g + head) * 32 + j) * 8), 0, AUX_SC1);
      }
      __builtin_amdgcn_s_setprio(0);
    }
    if (wave == 0) STAMP(4);
    asm volatile("" :: "v"(cpf));
    if (t3a) {
      float a[8];
  #pragma unroll
      for (int c = 0; c < 4; ++c) { a[2 * c] = bflo(wgu_r[c]); a[2 * c + 1] = bfhi(wgu_r[c]); }
      const float z = red8_b345(a, b3);
      ldf[L_CSP + wave * 64 + 8 * (lane & 7) + 4 * lb5 + 2 * lb4 + (lane >> 3 & 1)] = z;
      LGKM0();
      if (lane == 0) LDS_INC(F_CSC);
      if (wave == 4 || wave == 5) {
        while (flg[F_CSC] < 16u || flg[F_T] == 0u) __builtin_amdgcn_s_sleep(4);
        CBAR();
        float cs_ = 0.f;
  #pragma unroll
        for (int w = 0; w < 8; ++w) cs_ += ldf[L_CSP + ((wave - 4) * 8 + w) * 64 + lane];
        __builtin_amdgcn_raw_buffer_store_b32(enc(cs_), rs, (int)(WS_CS + (cb * 7 + rb) * 512 + (wave - 4) * 256 + lane * 4), 0, AUX_SC1);
      }
    }

    if (wave == 7) {
      while (flg[F_T] == 0u) __builtin_amdgcn_s_sleep(2);
      CBAR();
      __builtin_amdgcn_s_setprio(3);
      const unsigned t0 = lds[L_TAG], t1 = lds[L_TAG + 1];
      unsigned spins = 0;
      for (;;) {
        CBAR();
        const v4u r0 = __builtin_amdgcn_raw_buffer_load_b128(rs, (int)(WS_READY + lane * 16), 0, AUX_SC1);
        const v4u r1 = __builtin_amdgcn_raw_buffer_load_b128(rs, (int)(WS_READY + 1024 + lane * 16), 0, AUX_SC1);
        const bool ok = (r0[0] == t0) & (r0[1] == t1) & (r0[2] == t0) & (r0[3] == t1) & (r1[0] == t0) & (r1[1] == t1) & (r1[2] == t0) & (r1[3] == t1);
        if (__all(ok) || ++spins > SPIN_CAP) break;
        __builtin_amdgcn_s_sleep(8);
      }
      if (lane == 0) {
        flg[F_R] = 1u;
        if (blockIdx.x == 0) __builtin_amdgcn_raw_buffer_store_b64(v2u{t0, t1}, rs, (int)WS_E, 0, AUX_SC1);
      }
      if (t2) {
        v2u rec = {1u, 1u};
        bool ok = lane >= 32;
        spins = 0;
        for (;;) {
          CBAR();
          if (!ok) { rec = __builtin_amdgcn_raw_buffer_load_b64(rs, (int)(WS_ML + (hq * 32 + lane) * 8), 0, AUX_SC1); ok = (rec[0] != 0u) & (rec[1] != 0u); }
          if (__all(ok) || ++spins > SPIN_CAP) break;
          __builtin_amdgcn_s_sleep(8);
        }
        const float ms = lane < 32 ? dec(rec[0]) : -INFINITY, lsum = lane < 32 ? dec(rec[1]) : 0.f;
        const float M = wave_max(ms);
        const float e = lane < 32 ? __builtin_amdgcn_exp2f((ms - M) * 1.4426950408889634f) : 0.f;
        const float L = wave_sum(e * lsum);
        if (lane < 32) ldf[L_WTS + lane] = e / L;
        LGKM0();
        if (lane == 0) flg[F_W2] = 1u;
      }
      __builtin_amdgcn_s_setprio(0);
      STAMP(5);
    }
    if (wave == 6 && t3b) {
      while (flg[F_R] == 0u) __builtin_amdgcn_s_sleep(4);
      const int o0 = (int)(WS_CS + ((fidx >> 3) * 7 + (lane >> 4)) * 512 + (((lane >> 3) & 1) * 64 + 8 * (fidx & 7) + (lane & 7)) * 4);
      unsigned c0 = 0u, c1 = lane < 48 ? 0u : 0x80000000u;
      unsigned spins = 0;
      for (;;) {
        CBAR();
        if (c0 == 0u) c0 = __builtin_amdgcn_raw_buffer_load_b32(rs, o0, 0, AUX_SC1);
        if (c1 == 0u) c1 = __builtin_amdgcn_raw_buffer_load_b32(rs, o0 + 2048, 0, AUX_SC1);
        if (__all((c0 != 0u) & (c1 != 0u)) || ++spins > SPIN_CAP) break;
        __builtin_amdgcn_s_sleep(16);
      }
      float cs_ = dec(c0) + dec(c1);
      cs_ = tr16(cs_, cs_); cs_ = tr32(cs_, cs_);
      if (lane < 16) ldf[L_CST + lane] = cs_;
      LGKM0();
      if (lane == 0) flg[F_CS] = 1u;
    }
  } else {
    const int v = wave - 8;
    __builtin_amdgcn_s_setprio(3);
    const v2u ev = __builtin_amdgcn_raw_buffer_load_b64(rs, (int)WS_E, 0, AUX_SC1);
    if (v < 7) {
      const int r0 = 112 * (j & 3) + 16 * v + (lane >> 3);
      const v4u xv = *(const v4u*)(x + 8 * (lane < 56 ? lane : 55));
      const unsigned xr0 = (unsigned)x[r0], xr1 = (unsigned)x[r0 + 8];
      const u16* wqp = wq + (size_t)r0 * 4096 + 64 * (8 * g + (j >> 2)) + 8 * (lane & 7);
      const v4u W0 = ldnt(wqp), W1 = ldnt(wqp + (size_t)8 * 4096);
      CBAR();
      if (lane == 0) LDS_INC(F_EI);
      CBAR();
      float xf[8];
#pragma unroll
      for (int i = 0; i < 4; ++i) { xf[2 * i] = bflo(xv[i]); xf[2 * i + 1] = bfhi(xv[i]); }
      float sm = ((xf[0] + xf[1]) + (xf[2] + xf[3])) + ((xf[4] + xf[5]) + (xf[6] + xf[7]));
      sm = lane < 56 ? sm : 0.f;
      const float mean = wave_sum(sm) * (1.f / 448.f);
      float vs = 0.f;
#pragma unroll
      for (int i = 0; i < 8; ++i) { const float d = xf[i] - mean; vs += d * d; }
      vs = lane < 56 ? vs : 0.f;
      const float var = wave_sum(vs) * (1.f / 448.f);
      const float mb = rbf(mean), den = rbf(__builtin_sqrtf(rbf(rbf(var) + 1e-5f)));
      const float xn0 = rbf(rbf(bflo(xr0) - mb) / den), xn1 = rbf(rbf(bflo(xr1) - mb) / den);
      float acc[8];
#pragma unroll
      for (int c = 0; c < 4; ++c) {
        acc[2 * c] = fmaf(xn1, bflo(W1[c]), xn0 * bflo(W0[c]));
        acc[2 * c + 1] = fmaf(xn1, bfhi(W1[c]), xn0 * bfhi(W0[c]));
      }
      const float qv = red8_b345(acc, b3);
      ldf[L_QP + v * 64 + 8 * (lane & 7) + 4 * lb5 + 2 * lb4 + (lane >> 3 & 1)] = qv;
      LGKM0();
      unsigned oldq = 0;
      if (lane == 0) oldq = LDS_INC(F_QC);
      oldq = __builtin_amdgcn_readfirstlane(oldq);
      if (oldq == 6u) {
        CBAR();
        const u64 T = (((u64)ev[1] << 32) | ev[0]) + 1ull;
        float qs = 0.f;
#pragma unroll
        for (int w = 0; w < 7; ++w) qs += ldf[L_QP + w * 64 + lane];
        const unsigned qbits = (asu(qs) + 0x80u) & 0xffffff00u;
        v2u gr = {qbits | ((unsigned)T & 0xffu), (unsigned)(T >> 8)};
        __builtin_amdgcn_raw_buffer_store_b64(gr, rs, (int)(WS_QG + (8 * g + (j >> 2)) * 2048 + (j & 3) * 512 + lane * 8), 0, AUX_SC1);
        if (lane == 0) { lds[L_TAG] = (unsigned)T; lds[L_TAG + 1] = (unsigned)(T >> 32); LGKM0(); flg[F_QP] = 1u; }
        STAMP(1);
      }
    } else {
      const v4u z4 = {0u, 0u, 0u, 0u};
      __builtin_amdgcn_raw_buffer_store_b128(z4, rs, (int)(WS_PART + ((2 * g) * 32 + j) * 1024 + 16 * lane), 0, AUX_SC1);
      __builtin_amdgcn_raw_buffer_store_b128(z4, rs, (int)(WS_PART + ((2 * g + 1) * 32 + j) * 1024 + 16 * lane), 0, AUX_SC1);
      if (lane < 2) __builtin_amdgcn_raw_buffer_store_b64(v2u{0u, 0u}, rs, (int)(WS_ML + ((2 * g + lane) * 32 + j) * 8), 0, AUX_SC1);
      if (t2 && lane < 16) __builtin_amdgcn_raw_buffer_store_b128(z4, rs, (int)(WS_Y + (cb7 * 32 + hq * 2 + rh) * 256 + 16 * lane), 0, AUX_SC1);
      if (t3a) {
        if (lane < 32) __builtin_amdgcn_raw_buffer_store_b128(z4, rs, (int)(WS_CS + (cb * 7 + rb) * 512 + 16 * lane), 0, AUX_SC1);
        __builtin_amdgcn_raw_buffer_store_b128(z4, rs, (int)(WS_G + (cb * 7 + rb) * 1024 + 16 * lane), 0, AUX_SC1);
      }
      if (t3b) {
        __builtin_amdgcn_raw_buffer_store_b128(z4, rs, (int)(WS_F + fidx * 1792 + 16 * lane), 0, AUX_SC1);
        if (lane < 48) __builtin_amdgcn_raw_buffer_store_b128(z4, rs, (int)(WS_F + fidx * 1792 + 1024 + 16 * lane), 0, AUX_SC1);
      }
      CBAR();
      if (lane == 0) LDS_INC(F_EI);
      VM0();
      const u64 T = (((u64)ev[1] << 32) | ev[0]) + 1ull;
      if (lane == 0) {
        __builtin_amdgcn_raw_buffer_store_b64(v2u{(unsigned)T, (unsigned)(T >> 32)}, rs, (int)(WS_READY + blockIdx.x * 8), 0, AUX_SC1);
        flg[F_T] = 1u;
      }
      STAMP(3);
    }
    __builtin_amdgcn_s_setprio(0);
    const u16* vb = vc + ((size_t)g * 8192 + 256 * j + 32 * v + (lane >> 4)) * 256 + 8 * (lane & 15);
    v4u V[16];
    while (flg[F_KI0 + v] == 0u) __builtin_amdgcn_s_sleep(1);
    CBAR();
    if (t2) wo_r = ldnt(wo_p);
    if (t3a) wgu_r = ldnt(wgu_p);
    if (t3b && v < 2) {
      const int tc = (tid - 512) < 112 ? (tid - 512) : 111;
#pragma unroll
      for (int r = 0; r < 8; ++r) wd_r[r] = __builtin_nontemporal_load((const v2u*)(wd + (size_t)(8 * fidx + r) * 448 + 4 * tc));
    }
    CBAR();
#pragma unroll
    for (int i = 0; i < 16; ++i) { V[i] = ldnt(vb + (i >> 1) * 1024 + (i & 1) * 128); CBAR(); }
    __builtin_amdgcn_s_setprio(2);
    if (t3a) {
      float a[8];
#pragma unroll
      for (int c = 0; c < 4; ++c) { a[2 * c] = bflo(wgu_r[c]); a[2 * c + 1] = bfhi(wgu_r[c]); }
      const float z = red8_b345(a, b3);
      ldf[L_CSP + wave * 64 + 8 * (lane & 7) + 4 * lb5 + 2 * lb4 + (lane >> 3 & 1)] = z;
      LGKM0();
      if (lane == 0) LDS_INC(F_CSC);
    }
    while (flg[F_P0 + v] == 0u) __builtin_amdgcn_s_sleep(2);
    CBAR();
    const LAS v4u* pp = (const LAS v4u*)(lds + L_P + v * 64 + ((lane >> 3) & 1) * 32 + (lane >> 4) * 8);
    const v4u pa = pp[0], pb = pp[1];
    const unsigned hm = (lane & 1) ? 0xffff0000u : 0x0000ffffu;
    const int rsel = (lane >> 1) & 3;
    const unsigned m0 = rsel == 0 ? hm : 0u, m1 = rsel == 1 ? hm : 0u, m2 = rsel == 2 ? hm : 0u, m3 = rsel == 3 ? hm : 0u;
    f32x4 a0 = {0.f, 0.f, 0.f, 0.f}, a1 = {0.f, 0.f, 0.f, 0.f};
#pragma unroll
    for (int kg = 0; kg < 8; ++kg) {
      const unsigned pd = kg < 4 ? pa[kg & 3] : pb[kg & 3];
      const v4u A = {pd & m0, pd & m1, pd & m2, pd & m3};
      a0 = mfma(A, V[2 * kg], a0);
      a1 = mfma(A, V[2 * kg + 1], a1);
    }
    LAS f32x4* rp = (LAS f32x4*)(ldf + L_RED + v * 512 + (lane >> 5) * 256 + 8 * (lane & 15) + 4 * ((lane >> 4) & 1));
    rp[0] = a0; rp[32] = a1;
    LGKM0();
    unsigned oldv = 0;
    if (lane == 0) oldv = LDS_INC(F_VC);
    oldv = __builtin_amdgcn_readfirstlane(oldv);
    if (oldv == 7u) {
      CBAR();
      STAMP(6);
      const LAS f32x4* sp = (const LAS f32x4*)(ldf + L_RED + 4 * lane);
      f32x4 r0[8], r1[8];
#pragma unroll
      for (int w = 0; w < 8; ++w) { r0[w] = sp[w * 128]; r1[w] = sp[w * 128 + 64]; }
      const f32x4 s0 = ((r0[0] + r0[1]) + (r0[2] + r0[3])) + ((r0[4] + r0[5]) + (r0[6] + r0[7]));
      const f32x4 s1 = ((r1[0] + r1[1]) + (r1[2] + r1[3])) + ((r1[4] + r1[5]) + (r1[6] + r1[7]));
      v4u e0 = {enc(s0[0]), enc(s0[1]), enc(s0[2]), enc(s0[3])};
      v4u e1 = {enc(s1[0]), enc(s1[1]), enc(s1[2]), enc(s1[3])};
      __builtin_amdgcn_raw_buffer_store_b128(e0, rs, (int)(WS_PART + ((2 * g) * 32 + j) * 1024 + 16 * lane), 0, AUX_SC1);
      __builtin_amdgcn_raw_buffer_store_b128(e1, rs, (int)(WS_PART + ((2 * g + 1) * 32 + j) * 1024 + 16 * lane), 0, AUX_SC1);
      STAMP(7);
    }
  }
  if (t2) {
    {
      while (flg[F_VC] < 8u || flg[F_R] == 0u) __builtin_amdgcn_s_sleep(1);
      CBAR();
      v4u pa;
      unsigned spins = 0;
      const int off = (int)(WS_PART + (hq * 32 + (lane >> 1)) * 1024 + (128 * rh + 8 * wave + 4 * (lane & 1)) * 4);
      bool ok = false;
      __builtin_amdgcn_s_sleep(T2DELAY);
      for (;;) {
        CBAR();
        if (!ok) {
          pa = __builtin_amdgcn_raw_buffer_load_b128(rs, off, 0, AUX_SC1);
          ok = (pa[0] != 0u) & (pa[1] != 0u) & (pa[2] != 0u) & (pa[3] != 0u);
        }
        if (__all(ok) || ++spins > SPIN_CAP) break;
        __builtin_amdgcn_s_sleep(PSLEEP);
      }
      if (wave == 0) STAMP(8);
      if (wave == 15) STAMP(21);
      if (wave == 7) STAMP(22);
      while (flg[F_W2] == 0u) __builtin_amdgcn_s_sleep(1);
      CBAR();
      const float w = ldf[L_WTS + (lane >> 1)];
      const float u0 = tr32(w * dec(pa[0]), w * dec(pa[2])), u1 = tr32(w * dec(pa[1]), w * dec(pa[3]));
      float o = tr16(u0, u1);
      o += dpp<XOR2>(o); o += dpp<ROR8>(o); o += dpp<ROR4>(o);
      o = rbf(o);
      float y[8];
#pragma unroll
      for (int c = 0; c < 4; ++c) { y[2 * c] = o * bflo(wo_r[c]); y[2 * c + 1] = o * bfhi(wo_r[c]); }
#pragma unroll
      for (int c = 0; c < 4; ++c) y[c] = tr32(y[c], y[c + 4]);
#pragma unroll
      for (int c = 0; c < 2; ++c) y[c] = tr16(y[c], y[c + 2]);
      const float yy = trd<XOR1>(y[0], y[1], b0);
      ldf[L_RED2 + wave * 64 + 8 * ((lane >> 1) & 7) + 4 * lb5 + 2 * lb4 + (lane & 1)] = yy;
    }
    __syncthreads();
    if (wave == 0) {
      STAMP(18);
      const LAS f32x4* rp = (const LAS f32x4*)(ldf + L_RED2 + (lane >> 4) * 256 + 4 * (lane & 15));
      const f32x4 a = (rp[0] + rp[16]) + (rp[32] + rp[48]);
      const float u0 = tr32(a[0], a[2]), u1 = tr32(a[1], a[3]);
      const float z = tr16(u0, u1);
      __builtin_amdgcn_raw_buffer_store_b32(enc(z), rs, (int)(WS_Y + (cb7 * 32 + hq * 2 + rh) * 256 + (4 * (lane & 15) + 2 * lb5 + lb4) * 4), 0, AUX_SC1);
      STAMP(9);
    }
  }
  if (t3a) {
    const unsigned xraw = (unsigned)x[64 * rb + 8 * (wave & 7) + (lane >> 3)];
    if (wave >= 8) {
      v4u yv;
      unsigned spins = 0;
      bool ok = false;
      const int off = (int)(WS_Y + (rb * 32 + 4 * (wave - 8) + (lane >> 4)) * 256 + (lane & 15) * 16);
      __builtin_amdgcn_s_sleep(T3DELAY);
      for (;;) {
        CBAR();
        if (!ok) {
          yv = __builtin_amdgcn_raw_buffer_load_b128(rs, off, 0, AUX_SC1);
          ok = (yv[0] != 0u) & (yv[1] != 0u) & (yv[2] != 0u) & (yv[3] != 0u);
        }
        if (__all(ok) || ++spins > SPIN_CAP) break;
        __builtin_amdgcn_s_sleep(PSLEEP);
      }
      if (wave == 8) STAMP(10);
      if (wave == 15) STAMP(19);
      if (wave == 12) STAMP(20);
      const float u0 = tr32(dec(yv[0]), dec(yv[2])), u1 = tr32(dec(yv[1]), dec(yv[3]));
      ldf[L_HP + (wave - 8) * 64 + 4 * (lane & 15) + 2 * lb5 + lb4] = tr16(u0, u1);
    }
    __syncthreads();
    if (wave == 0) STAMP(14);
    {
      const int row = 8 * (wave & 7) + (lane >> 3);
      float t8[8];
#pragma unroll
      for (int w = 0; w < 8; ++w) t8[w] = ldf[L_HP + w * 64 + row];
      const float at = ((t8[0] + t8[1]) + (t8[2] + t8[3])) + ((t8[4] + t8[5]) + (t8[6] + t8[7]));
      const float h = rbf(bflo(xraw) + rbf(at));
      float a[8];
#pragma unroll
      for (int c = 0; c < 4; ++c) { a[2 * c] = h * bflo(wgu_r[c]); a[2 * c + 1] = h * bfhi(wgu_r[c]); }
      const float z = red8_b345(a, b3);
      ldf[L_RED3 + wave * 64 + 8 * (lane & 7) + 4 * lb5 + 2 * lb4 + (lane >> 3 & 1)] = z;
      if (wave < 8 && (lane & 7) == 0) ldf[L_QP + row] = h;
    }
    __syncthreads();
    if (wave == 0) STAMP(15);
    if (wave < 2) {
      float t8[8];
#pragma unroll
      for (int w = 0; w < 8; ++w) t8[w] = ldf[L_RED3 + (wave * 8 + w) * 64 + lane];
      const float z = ((t8[0] + t8[1]) + (t8[2] + t8[3])) + ((t8[4] + t8[5]) + (t8[6] + t8[7]));
      __builtin_amdgcn_raw_buffer_store_b32(enc(z), rs, (int)(WS_G + (cb * 7 + rb) * 1024 + (lane >> 3) * 128 + (8 * wave + (lane & 7)) * 4), 0, AUX_SC1);
    } else if (wave == 2) {
      const float h = ldf[L_QP + lane];
      const float piv = asf((unsigned)__builtin_amdgcn_readfirstlane((int)asu(h)));
      const float d = h - piv;
      float st = tr32(d, d * d);
      st = tr16(st, st); st += dpp<XOR1>(st); st += dpp<XOR2>(st); st += dpp<ROR8>(st); st += dpp<ROR4>(st);
      const int k = lane >> 3;
      if (k == 0 || k == 1 || k == 4) {
        const float val = k == 0 ? piv : st;
        __builtin_amdgcn_raw_buffer_store_b32(enc(val), rs, (int)(WS_G + (cb * 7 + rb) * 1024 + (lane & 7) * 128 + (16 + (k == 4 ? 2 : k)) * 4), 0, AUX_SC1);
      }
    }
    if (wave == 0) STAMP(16);
  }
  if (t3b) {
    if (wave == 8 || wave == 9) {
      if (!t3a) { while (flg[F_VC] < 8u || flg[F_R] == 0u) __builtin_amdgcn_s_sleep(1); __builtin_amdgcn_s_sleep(40); }
      v4u gv = {1u, 1u, 1u, 1u};
      bool ok = (lane >= 56) || ((lane & 7) > 4);
      unsigned spins = 0;
      const int off = (int)(WS_G + ((fidx >> 3) * 7 + (lane >> 3)) * 1024 + (fidx & 7) * 128 + (lane & 7) * 16);
      __builtin_amdgcn_s_sleep(T3BDELAY);
      for (;;) {
        CBAR();
        if (!ok) {
          gv = __builtin_amdgcn_raw_buffer_load_b128(rs, off, 0, AUX_SC1);
          ok = (gv[0] != 0u) & (gv[1] != 0u) & (gv[2] != 0u) & ((gv[3] != 0u) | ((lane & 7) == 4));
        }
        if (__all(ok) || ++spins > SPIN_CAP) break;
        __builtin_amdgcn_s_sleep(PSLEEP);
      }
      if (wave == 8) STAMP(17);
      while (flg[F_CS] == 0u) __builtin_amdgcn_s_sleep(1);
      CBAR();
      const bool live = lane < 56;
      const bool isst = (lane & 7) == 4;
      const float piv = dec(gv[0]), s1 = dec(gv[1]), s2 = dec(gv[2]);
      const float dm = s1 * (1.f / 64.f);
      const float mw = piv + dm, m2 = s2 - s1 * dm;
      float a0 = live ? (isst ? mw : dec(gv[0])) : 0.f, a1 = live ? dec(gv[1]) : 0.f, a2 = live ? dec(gv[2]) : 0.f, a3 = live ? dec(gv[3]) : 0.f;
      a0 = tr32(a0, a0); a1 = tr32(a1, a1); a2 = tr32(a2, a2); a3 = tr32(a3, a3);
      a0 = tr16(a0, a0); a1 = tr16(a1, a1); a2 = tr16(a2, a2); a3 = tr16(a3, a3);
      a0 += dpp<ROR8>(a0); a1 += dpp<ROR8>(a1); a2 += dpp<ROR8>(a2); a3 += dpp<ROR8>(a3);
      const float mean = asf((unsigned)__builtin_amdgcn_readlane((int)asu(a0), 4)) * (1.f / 7.f);
      const float dd = mw - mean;
      float vv = (live && isst) ? m2 + 64.f * dd * dd : 0.f;
      vv = tr32(vv, vv); vv = tr16(vv, vv); vv += dpp<ROR8>(vv);
      const float var = fmaxf(asf((unsigned)__builtin_amdgcn_readlane((int)asu(vv), 4)) * (1.f / 448.f), 0.f);
      const float mb = rbf(mean), den = rbf(__builtin_sqrtf(rbf(rbf(var) + 1e-5f)));
      const float rden = 1.f / den;
      const f32x4 cs4 = *(const LAS f32x4*)(ldf + L_CST + 4 * (lane & 3));
      const float z0 = rbf((a0 - mb * cs4[0]) * rden), z1 = rbf((a1 - mb * cs4[1]) * rden), z2 = rbf((a2 - mb * cs4[2]) * rden), z3 = rbf((a3 - mb * cs4[3]) * rden);
      const float u0 = dpp<XOR2>(z0), u1 = dpp<XOR2>(z1), u2 = dpp<XOR2>(z2), u3 = dpp<XOR2>(z3);
      float av[4];
      {
        const float zz[4] = {z0, z1, z2, z3}, uu[4] = {u0, u1, u2, u3};
#pragma unroll
        for (int i = 0; i < 4; ++i) {
          const float z = zz[i];
          const float t = 0.7978845608028654f * (z + 0.044715f * z * z * z);
          const float e2 = __builtin_amdgcn_exp2f(t * 2.8853900817779268f);
          const float th = 1.f - 2.f * __builtin_amdgcn_rcpf(e2 + 1.f);
          av[i] = rbf(rbf(0.5f * z * (1.f + th)) * uu[i]);
        }
      }
      f32x4 f = {0.f, 0.f, 0.f, 0.f};
#pragma unroll
      for (int r = 0; r < 8; ++r) {
        const float ar = asf((unsigned)__builtin_amdgcn_readlane((int)asu(av[r & 3]), r >> 2));
        f[0] = fmaf(ar, bflo(wd_r[r][0]), f[0]); f[1] = fmaf(ar, bfhi(wd_r[r][0]), f[1]);
        f[2] = fmaf(ar, bflo(wd_r[r][1]), f[2]); f[3] = fmaf(ar, bfhi(wd_r[r][1]), f[3]);
      }
      if (tid - 512 < 112) {
        v4u e = {enc(f[0]), enc(f[1]), enc(f[2]), enc(f[3])};
        __builtin_amdgcn_raw_buffer_store_b128(e, rs, (int)(WS_F + fidx * 1792 + 16 * (tid - 512)), 0, AUX_SC1);
      }
      if (wave == 8) STAMP(11);
    }
  } else if (t4) {
    const int rid = j;
    {
      const int off = wave < 14 ? (int)(WS_F + (16 * wave + (lane >> 2)) * 1792 + (16 * rid + 4 * (lane & 3)) * 4)
                                : (int)(WS_Y + ((rid >> 2) * 32 + 16 * (wave - 14) + (lane >> 2)) * 256 + (16 * (rid & 3) + 4 * (lane & 3)) * 4);
      v4u pv;
      unsigned spins = 0;
      bool ok = false;
      if (wave < 14) { while (flg[F_Y4] < 2u) __builtin_amdgcn_s_sleep(1); __builtin_amdgcn_s_sleep(T4DELAY); }
      else __builtin_amdgcn_s_sleep(T3DELAY);
      for (;;) {
        CBAR();
        if (!ok) {
          pv = __builtin_amdgcn_raw_buffer_load_b128(rs, off, 0, AUX_SC1);
          ok = (pv[0] != 0u) & (pv[1] != 0u) & (pv[2] != 0u) & (pv[3] != 0u);
        }
        if (__all(ok) || ++spins > SPIN_CAP) break;
        __builtin_amdgcn_s_sleep(PSLEEP);
      }
      if (wave >= 14 && lane == 0) LDS_INC(F_Y4);
      if (wave == 0) STAMP(12);
      const float a0 = tr32(dec(pv[0]), dec(pv[2])), a1 = tr32(dec(pv[1]), dec(pv[3]));
      float z = tr16(a0, a1);
      z += dpp<ROR8>(z); z += dpp<ROR4>(z);
      const int col = 4 * (lane & 3) + 2 * lb5 + lb4;
      if ((lane & 12) == 0) ldf[(wave < 14 ? L_RED4 + wave * 16 : L_H4 + (wave - 14) * 16) + col] = z;
    }
    const unsigned xraw = (unsigned)x[16 * rid + (tid & 15)];
    __syncthreads();
    if (tid < 16) {
      float t[14];
#pragma unroll
      for (int w = 0; w < 14; ++w) t[w] = ldf[L_RED4 + w * 16 + tid];
      const float f = (((t[0] + t[1]) + (t[2] + t[3])) + ((t[4] + t[5]) + (t[6] + t[7]))) + (((t[8] + t[9]) + (t[10] + t[11])) + (t[12] + t[13]));
      const float h = rbf(bflo(xraw) + rbf(ldf[L_H4 + tid] + ldf[L_H4 + 16 + tid]));
      out[16 * rid + tid] = (u16)f2bf(h + rbf(f));
    }
    if (wave == 0) STAMP(13);
  }
}

template <int NV> __global__ void __launch_bounds__(1024) spin_k(unsigned* sink, int lds_touch) {
  extern __shared__ unsigned dl[];
  const u64 t0 = __builtin_amdgcn_s_memrealtime();
  while (__builtin_amdgcn_s_memrealtime() - t0 < 2000ull) __builtin_amdgcn_s_sleep(1);
  if (lds_touch == 12345) { dl[threadIdx.x] = 1; sink[0] = dl[0]; }
}
extern "C" void kernel_launch(void* const* d_in, const int* in_sizes, int n_in,
                              void* d_out, int out_size, void* d_ws, size_t ws_size,
                              hipStream_t stream) {
  spin_k<0><<<256, 256, 0, stream>>>((unsigned*)d_ws + (8u << 20), 0);
  fused_decode<<<256, 1024, 0, stream>>>((const u16*)d_in[0], (const u16*)d_in[1], (const u16*)d_in[2], (const u16*)d_in[3],
                                         (const u16*)d_in[4], (const u16*)d_in[6], (const u16*)d_in[7], (const u16*)d_in[8],
                                         (u16*)d_out, (unsigned char*)d_ws);
}
```

```cpp
#define DIAG 31
#include <hip/hip_runtime.h>
#include <stdint.h>

typedef unsigned short u16;
typedef unsigned long long u64;
typedef unsigned v4u __attribute__((ext_vector_type(4)));
typedef unsigned v2u __attribute__((ext_vector_type(2)));
typedef float f32x4 __attribute__((ext_vector_type(4)));
typedef short bf16x8 __attribute__((ext_vector_type(8)));
#define LAS __attribute__((address_space(3)))

#define STAMPS 0

constexpr int AUX_SC1 = 16;
constexpr unsigned WS_E = 0;
constexpr unsigned WS_READY = 4096;
constexpr unsigned WS_QG = 8192;
constexpr unsigned WS_ML = 139264;
constexpr unsigned WS_Y = 143360;
constexpr unsigned WS_CS = 200704;
constexpr unsigned WS_G = 301056;
constexpr unsigned WS_PART = 501760;
constexpr unsigned WS_F = 1026048;
constexpr unsigned WS_END = 1427456;
constexpr unsigned WS_STAMP = 2u << 20;
constexpr unsigned SPIN_CAP = 1u << 17;
#define CODEPF 1
#define CODEBYTES 13824
#define QDELAY 16
#define T2DELAY 8
#define T3DELAY 8
#define T3BDELAY 8
#define T4DELAY 40
#define PSLEEP 4

__device__ __forceinline__ float asf(unsigned u) { return __builtin_bit_cast(float, u); }
__device__ __forceinline__ unsigned asu(float f) { return __builtin_bit_cast(unsigned, f); }
__device__ __forceinline__ float bflo(unsigned w) { return asf(w << 16); }
__device__ __forceinline__ float bfhi(unsigned w) { return asf(w & 0xffff0000u); }
__device__ __forceinline__ unsigned f2bf(float f) { __bf16 b = (__bf16)f; return (unsigned)__builtin_bit_cast(u16, b); }
__device__ __forceinline__ float rbf(float f) { return asf(f2bf(f) << 16); }
__device__ __forceinline__ unsigned enc(float f) { unsigned u = asu(f); u = (u == 0x80000000u) ? 0u : u; return u ^ 0x80000000u; }
__device__ __forceinline__ float dec(unsigned u) { return asf(u ^ 0x80000000u); }

template <int CTRL> __device__ __forceinline__ float dpp(float x) {
  return asf((unsigned)__builtin_amdgcn_mov_dpp((int)asu(x), CTRL, 0xf, 0xf, true));
}
constexpr int XOR1 = 0xB1, XOR2 = 0x4E, ROR4 = 0x124, ROR8 = 0x128;
__device__ __forceinline__ float tr32(float a, float b) {
  auto r = __builtin_amdgcn_permlane32_swap(asu(a), asu(b), false, false);
  return asf(r[0]) + asf(r[1]);
}
__device__ __forceinline__ float tr16(float a, float b) {
  auto r = __builtin_amdgcn_permlane16_swap(asu(a), asu(b), false, false);
  return asf(r[0]) + asf(r[1]);
}
template <int CTRL> __device__ __forceinline__ float trd(float a, float b, bool bit) {
  const float t = bit ? a : b, k = bit ? b : a;
  return k + dpp<CTRL>(t);
}
__device__ __forceinline__ float mx32(float a) {
  auto r = __builtin_amdgcn_permlane32_swap(asu(a), asu(a), false, false);
  return fmaxf(asf(r[0]), asf(r[1]));
}
__device__ __forceinline__ float mx16(float a) {
  auto r = __builtin_amdgcn_permlane16_swap(asu(a), asu(a), false, false);
  return fmaxf(asf(r[0]), asf(r[1]));
}
__device__ __forceinline__ float wave_sum(float x) {
  x += dpp<XOR1>(x); x += dpp<XOR2>(x); x += dpp<ROR8>(x); x += dpp<ROR4>(x);
  x = tr16(x, x); x = tr32(x, x);
  return x;
}
__device__ __forceinline__ float wave_max(float x) {
  x = fmaxf(x, dpp<XOR1>(x)); x = fmaxf(x, dpp<XOR2>(x)); x = fmaxf(x, dpp<ROR8>(x)); x = fmaxf(x, dpp<ROR4>(x));
  x = mx16(x); x = mx32(x);
  return x;
}
__device__ __forceinline__ float red8_b345(float (&a)[8], bool b3) {
#pragma unroll
  for (int c = 0; c < 4; ++c) a[c] = tr32(a[c], a[c + 4]);
#pragma unroll
  for (int c = 0; c < 2; ++c) a[c] = tr16(a[c], a[c + 2]);
  return trd<ROR8>(a[0], a[1], b3);
}
#define KVPOL 1
__device__ __forceinline__ v4u ldnt(const u16* p) {
  return __builtin_nontemporal_load((const v4u*)p);
}
__device__ __forceinline__ f32x4 mfma(v4u a, v4u b, f32x4 c) {
  return __builtin_amdgcn_mfma_f32_16x16x32_bf16(__builtin_bit_cast(bf16x8, a), __builtin_bit_cast(bf16x8, b), c, 0, 0, 0);
}
#define LGKM0() asm volatile("s_waitcnt lgkmcnt(0)" ::: "memory")
#define VM0() asm volatile("s_waitcnt vmcnt(0)" ::: "memory")
#define CBAR() asm volatile("" ::: "memory")
#define LDS_INC(f) __hip_atomic_fetch_add((LAS unsigned*)(lds + L_FLAG + (f)), 1u, __ATOMIC_RELAXED, __HIP_MEMORY_SCOPE_WORKGROUP)

constexpr int L_Q = 0;
constexpr int L_QP = 256;
constexpr int L_P = 704;
constexpr int L_MW = 1216;
constexpr int L_LW = 1232;
constexpr int L_WTS = 1248;
constexpr int L_FLAG = 1280;
constexpr int L_TAG = 1312;
constexpr int L_CST = 1328;
constexpr int L_RED2 = 1344;
constexpr int L_HP = 2368;
constexpr int L_RED3 = 2880;
constexpr int L_CSP = 3904;
constexpr int L_RED4 = 4928;
constexpr int L_H4 = 5152;
constexpr int L_RED = 5184;
constexpr int L_TOTAL = L_RED + 4096;
constexpr int F_QD = 0, F_R = 1, F_MC = 2, F_LC = 3, F_VC = 4, F_T = 5, F_QC = 6, F_EI = 7, F_P0 = 8, F_KI0 = 16,
              F_Y4 = 24, F_W2 = 25, F_QP = 26, F_CSC = 27, F_CS = 28;

__global__ void __launch_bounds__(1024) fused_decode(const u16* __restrict__ x, const u16* __restrict__ kc, const u16* __restrict__ vc,
                                                     const u16* __restrict__ wq, const u16* __restrict__ wo, const u16* __restrict__ wg,
                                                     const u16* __restrict__ wu, const u16* __restrict__ wd, u16* __restrict__ out,
                                                     unsigned char* __restrict__ ws) {
  __shared__ __attribute__((aligned(16))) unsigned lds_raw[L_TOTAL];
  LAS unsigned* lds = (LAS unsigned*)lds_raw;
  LAS float* ldf = (LAS float*)lds_raw;
  volatile LAS unsigned* flg = (volatile LAS unsigned*)(lds + L_FLAG);
  const int tid = threadIdx.x, lane = tid & 63, wave = __builtin_amdgcn_readfirstlane(tid >> 6);
  const int g = blockIdx.x & 7, j = blockIdx.x >> 3;
  const bool t2 = j < 28;
  const int jj = t2 ? j : 0;
  const int hq = 2 * g + jj / 14, rh = (jj % 14) / 7, cb7 = jj % 7;
  const bool t3a = (g < 7) && t2;
  const int cb = 4 * g + jj / 7, rb = jj % 7;
  const bool t3b = g < 7;
  const int fidx = 32 * g + j;
  const bool t4 = (g == 7) && t2;
  const __amdgpu_buffer_rsrc_t rs = __builtin_amdgcn_make_buffer_rsrc((void*)ws, 0, (int)WS_END, 0x00020000);
  __shared__ u64 stl[32];
  const u64 tstart = __builtin_amdgcn_s_memrealtime();
  if (tid < 32) stl[tid] = tstart;
#define STAMP(i) do { if (lane == 0) stl[i] = __builtin_amdgcn_s_memrealtime(); } while (0)
  unsigned cpf = 0u;
  if (wave < 2) {
    const u64 pc = (u64)__builtin_amdgcn_s_getpc();
    const int o = (wave * 64 + lane) * 128;
    if (o < CODEBYTES) cpf = *(const __attribute__((address_space(1))) unsigned*)(pc + o);
  }
  if (tid < 32) lds[L_FLAG + tid] = 0u;
  __syncthreads();

  const bool b0 = lane & 1, b1 = lane & 2, b3 = lane & 8;
  const int lb4 = (lane >> 4) & 1, lb5 = (lane >> 5) & 1;
  v4u wo_r, wgu_r;
  v2u wd_r[8];
  const u16* wo_p = wo + (size_t)(hq * 256 + 128 * rh + 8 * wave + 4 * (lane & 1) + 2 * lb5 + lb4) * 448 + 64 * cb7 + 8 * ((lane >> 1) & 7);
  const u16* wgu_p = (wave < 8 ? wg : wu) + (size_t)(64 * rb + 8 * (wave & 7) + (lane >> 3)) * 1792 + 64 * cb + 8 * (lane & 7);

  if (wave < 8) {
    if (wave == 0) STAMP(0);
    const u16* kb = kc + ((size_t)g * 8192 + 256 * j + 32 * wave + ((lane >> 2) & 3)) * 256 + 8 * ((lane & 3) + 4 * (lane >> 4));
    v4u K[16];
    while (flg[F_EI] < 8u) __builtin_amdgcn_s_sleep(1);
    CBAR();
#pragma unroll
    for (int i = 0; i < 16; ++i) { K[i] = ldnt(kb + (i >> 1) * 1024 + (i & 1) * 128); CBAR(); }
    if (t2) wo_r = ldnt(wo_p);
    if (t3a) wgu_r = ldnt(wgu_p);
    CBAR();
    if (lane == 0) flg[F_KI0 + wave] = 1u;
    {
      while (flg[F_QP] == 0u) __builtin_amdgcn_s_sleep(1);
      CBAR();
      const unsigned tl = lds[L_TAG] & 0xffu, th = (lds[L_TAG] >> 8) | (lds[L_TAG + 1] << 24);
      __builtin_amdgcn_s_setprio(3);
      v4u qa, qb;
      unsigned spins = 0;
      bool ok = false;
      const int off = (int)(WS_QG + (8 * g + wave) * 2048 + (lane >> 5) * 512 + (lane & 31) * 16);
      __builtin_amdgcn_s_sleep(QDELAY);
      for (;;) {
        CBAR();
        if (!ok) {
          qa = __builtin_amdgcn_raw_buffer_load_b128(rs, off, 0, AUX_SC1);
          qb = __builtin_amdgcn_raw_buffer_load_b128(rs, off + 1024, 0, AUX_SC1);
          ok = ((qa[0] & 0xffu) == tl) & (qa[1] == th) & ((qa[2] & 0xffu) == tl) & (qa[3] == th) &
               ((qb[0] & 0xffu) == tl) & (qb[1] == th) & ((qb[2] & 0xffu) == tl) & (qb[3] == th);
        }
        if (__all(ok) || ++spins > SPIN_CAP) break;
        __builtin_amdgcn_s_sleep(8);
      }
      const float c0 = asf(qa[0] & 0xffffff00u) + asf(qb[0] & 0xffffff00u), c1 = asf(qa[2] & 0xffffff00u) + asf(qb[2] & 0xffffff00u);
      const float s0 = tr32(c0, c0), s1 = tr32(c1, c1);
      if (lane < 32) lds[L_Q + 32 * wave + lane] = f2bf(s0) | (f2bf(s1) << 16);
      LGKM0();
      if (lane == 0) LDS_INC(F_QD);
      __builtin_amdgcn_s_setprio(0);
      if (wave == 0) STAMP(2);
    }
    while (flg[F_QD] < 8u) __builtin_amdgcn_s_sleep(1);
    CBAR();
    v4u qf[2];
    {
      const int head = (lane >> 2) & 1;
      const LAS v4u* qp = (const LAS v4u*)(lds + L_Q + head * 128 + 4 * ((lane & 3) + 4 * (lane >> 4)));
      qf[0] = qp[0]; qf[1] = qp[16];
    }
    const int mykg = (lane & 3) | (((lane >> 3) & 1) << 2);
    float s = 0.f;
#pragma unroll
    for (int kg = 0; kg < 8; ++kg) {
      f32x4 acc = {0.f, 0.f, 0.f, 0.f};
      acc = mfma(K[2 * kg], qf[0], acc);
      acc = mfma(K[2 * kg + 1], qf[1], acc);
      float v = b1 ? (b0 ? acc[3] : acc[2]) : (b0 ? acc[1] : acc[0]);
      v += dpp<XOR1>(v); v += dpp<XOR2>(v);
      s = (mykg == kg) ? v : s;
    }
    s = rbf(s) * 0.0625f;
    float m = s;
    m = fmaxf(m, dpp<XOR1>(m)); m = fmaxf(m, dpp<XOR2>(m)); m = fmaxf(m, dpp<ROR8>(m)); m = mx16(m); m = mx32(m);
    if ((lane & ~4) == 0) ldf[L_MW + wave * 2 + (lane >> 2)] = m;
    LGKM0();
    if (lane == 0) LDS_INC(F_MC);
    while (flg[F_MC] < 8u) __builtin_amdgcn_s_sleep(1);
    CBAR();
    {
      const int head = (lane >> 2) & 1;
      float mm = ldf[L_MW + head];
#pragma unroll
      for (int w = 1; w < 8; ++w) mm = fmaxf(mm, ldf[L_MW + 2 * w + head]);
      m = mm;
    }
    const float p = __builtin_amdgcn_exp2f((s - m) * 1.4426950408889634f);
    const unsigned pb = f2bf(p);
    lds[L_P + wave * 64 + ((lane >> 2) & 1) * 32 + (lane >> 4) * 8 + mykg] = pb | (pb << 16);
    float l = asf(pb << 16);
    l += dpp<XOR1>(l); l += dpp<XOR2>(l); l += dpp<ROR8>(l); l = tr16(l, l); l = tr32(l, l);
    if ((lane & ~4) == 0) ldf[L_LW + wave * 2 + (lane >> 2)] = l;
    LGKM0();
    unsigned old = 0;
    if (lane == 0) { flg[F_P0 + wave] = 1u; old = LDS_INC(F_LC); }
    old = __builtin_amdgcn_readfirstlane(old);
    if (old == 7u) {
      while (flg[F_T] == 0u) __builtin_amdgcn_s_sleep(1);
      CBAR();
      __builtin_amdgcn_s_setprio(3);
      if ((lane & ~4) == 0) {
        const int head = lane >> 2;
        float ls = 0.f;
#pragma unroll
        for (int w = 0; w < 8; ++w) ls += ldf[L_LW + 2 * w + head];
        v2u rec = {enc(m), enc(ls)};
        __builtin_amdgcn_raw_buffer_store_b64(rec, rs, (int)(WS_ML + ((2 * g + head) * 32 + j) * 8), 0, AUX_SC1);
      }
      __builtin_amdgcn_s_setprio(0);
    }
    if (wave == 0) STAMP(4);
    asm volatile("" :: "v"(cpf));
    if (t3a) {
      float a[8];
  #pragma unroll
      for (int c = 0; c < 4; ++c) { a[2 * c] = bflo(wgu_r[c]); a[2 * c + 1] = bfhi(wgu_r[c]); }
      const float z = red8_b345(a, b3);
      ldf[L_CSP + wave * 64 + 8 * (lane & 7) + 4 * lb5 + 2 * lb4 + (lane >> 3 & 1)] = z;
      LGKM0();
      if (lane == 0) LDS_INC(F_CSC);
      if (wave == 4 || wave == 5) {
        while (flg[F_CSC] < 16u || flg[F_T] == 0u) __builtin_amdgcn_s_sleep(4);
        CBAR();
        float cs_ = 0.f;
  #pragma unroll
        for (int w = 0; w < 8; ++w) cs_ += ldf[L_CSP + ((wave - 4) * 8 + w) * 64 + lane];
        __builtin_amdgcn_raw_buffer_store_b32(enc(cs_), rs, (int)(WS_CS + (cb * 7 + rb) * 512 + (wave - 4) * 256 + lane * 4), 0, AUX_SC1);
      }
    }

    if (wave == 7) {
      while (flg[F_T] == 0u) __builtin_amdgcn_s_sleep(2);
      CBAR();
      __builtin_amdgcn_s_setprio(3);
      const unsigned t0 = lds[L_TAG], t1 = lds[L_TAG + 1];
      unsigned spins = 0;
      for (;;) {
        CBAR();
        const v4u r0 = __builtin_amdgcn_raw_buffer_load_b128(rs, (int)(WS_READY + lane * 16), 0, AUX_SC1);
        const v4u r1 = __builtin_amdgcn_raw_buffer_load_b128(rs, (int)(WS_READY + 1024 + lane * 16), 0, AUX_SC1);
        const bool ok = (r0[0] == t0) & (r0[1] == t1) & (r0[2] == t0) & (r0[3] == t1) & (r1[0] == t0) & (r1[1] == t1) & (r1[2] == t0) & (r1[3] == t1);
        if (__all(ok) || ++spins > SPIN_CAP) break;
        __builtin_amdgcn_s_sleep(8);
      }
      if (lane == 0) {
        flg[F_R] = 1u;
        if (blockIdx.x == 0) __builtin_amdgcn_raw_buffer_store_b64(v2u{t0, t1}, rs, (int)WS_E, 0, AUX_SC1);
      }
      if (t2) {
        v2u rec = {1u, 1u};
        bool ok = lane >= 32;
        spins = 0;
        for (;;) {
          CBAR();
          if (!ok) { rec = __builtin_amdgcn_raw_buffer_load_b64(rs, (int)(WS_ML + (hq * 32 + lane) * 8), 0, AUX_SC1); ok = (rec[0] != 0u) & (rec[1] != 0u); }
          if (__all(ok) || ++spins > SPIN_CAP) break;
          __builtin_amdgcn_s_sleep(8);
        }
        const float ms = lane < 32 ? dec(rec[0]) : -INFINITY, lsum = lane < 32 ? dec(rec[1]) : 0.f;
        const float M = wave_max(ms);
        const float e = lane < 32 ? __builtin_amdgcn_exp2f((ms - M) * 1.4426950408889634f) : 0.f;
        const float L = wave_sum(e * lsum);
        if (lane < 32) ldf[L_WTS + lane] = e / L;
        LGKM0();
        if (lane == 0) flg[F_W2] = 1u;
      }
      __builtin_amdgcn_s_setprio(0);
      STAMP(5);
    }
    if (wave == 6 && t3b) {
      while (flg[F_R] == 0u) __builtin_amdgcn_s_sleep(4);
      const int o0 = (int)(WS_CS + ((fidx >> 3) * 7 + (lane >> 4)) * 512 + (((lane >> 3) & 1) * 64 + 8 * (fidx & 7) + (lane & 7)) * 4);
      unsigned c0 = 0u, c1 = lane < 48 ? 0u : 0x80000000u;
      unsigned spins = 0;
      for (;;) {
        CBAR();
        if (c0 == 0u) c0 = __builtin_amdgcn_raw_buffer_load_b32(rs, o0, 0, AUX_SC1);
        if (c1 == 0u) c1 = __builtin_amdgcn_raw_buffer_load_b32(rs, o0 + 2048, 0, AUX_SC1);
        if (__all((c0 != 0u) & (c1 != 0u)) || ++spins > SPIN_CAP) break;
        __builtin_amdgcn_s_sleep(16);
      }
      float cs_ = dec(c0) + dec(c1);
      cs_ = tr16(cs_, cs_); cs_ = tr32(cs_, cs_);
      if (lane < 16) ldf[L_CST + lane] = cs_;
      LGKM0();
      if (lane == 0) flg[F_CS] = 1u;
    }
  } else {
    const int v = wave - 8;
    __builtin_amdgcn_s_setprio(3);
    const v2u ev = __builtin_amdgcn_raw_buffer_load_b64(rs, (int)WS_E, 0, AUX_SC1);
    if (v < 7) {
      const int r0 = 112 * (j & 3) + 16 * v + (lane >> 3);
      const v4u xv = *(const v4u*)(x + 8 * (lane < 56 ? lane : 55));
      const unsigned xr0 = (unsigned)x[r0], xr1 = (unsigned)x[r0 + 8];
      const u16* wqp = wq + (size_t)r0 * 4096 + 64 * (8 * g + (j >> 2)) + 8 * (lane & 7);
      const v4u W0 = ldnt(wqp), W1 = ldnt(wqp + (size_t)8 * 4096);
      CBAR();
      if (lane == 0) LDS_INC(F_EI);
      CBAR();
      float xf[8];
#pragma unroll
      for (int i = 0; i < 4; ++i) { xf[2 * i] = bflo(xv[i]); xf[2 * i + 1] = bfhi(xv[i]); }
      float sm = ((xf[0] + xf[1]) + (xf[2] + xf[3])) + ((xf[4] + xf[5]) + (xf[6] + xf[7]));
      sm = lane < 56 ? sm : 0.f;
      const float mean = wave_sum(sm) * (1.f / 448.f);
      float vs = 0.f;
#pragma unroll
      for (int i = 0; i < 8; ++i) { const float d = xf[i] - mean; vs += d * d; }
      vs = lane < 56 ? vs : 0.f;
      const float var = wave_sum(vs) * (1.f / 448.f);
      const float mb = rbf(mean), den = rbf(__builtin_sqrtf(rbf(rbf(var) + 1e-5f)));
      const float xn0 = rbf(rbf(bflo(xr0) - mb) / den), xn1 = rbf(rbf(bflo(xr1) - mb) / den);
      float acc[8];
#pragma unroll
      for (int c = 0; c < 4; ++c) {
        acc[2 * c] = fmaf(xn1, bflo(W1[c]), xn0 * bflo(W0[c]));
        acc[2 * c + 1] = fmaf(xn1, bfhi(W1[c]), xn0 * bfhi(W0[c]));
      }
      const float qv = red8_b345(acc, b3);
      ldf[L_QP + v * 64 + 8 * (lane & 7) + 4 * lb5 + 2 * lb4 + (lane >> 3 & 1)] = qv;
      LGKM0();
      unsigned oldq = 0;
      if (lane == 0) oldq = LDS_INC(F_QC);
      oldq = __builtin_amdgcn_readfirstlane(oldq);
      if (oldq == 6u) {
        CBAR();
        const u64 T = (((u64)ev[1] << 32) | ev[0]) + 1ull;
        float qs = 0.f;
#pragma unroll
        for (int w = 0; w < 7; ++w) qs += ldf[L_QP + w * 64 + lane];
        const unsigned qbits = (asu(qs) + 0x80u) & 0xffffff00u;
        v2u gr = {qbits | ((unsigned)T & 0xffu), (unsigned)(T >> 8)};
        __builtin_amdgcn_raw_buffer_store_b64(gr, rs, (int)(WS_QG + (8 * g + (j >> 2)) * 2048 + (j & 3) * 512 + lane * 8), 0, AUX_SC1);
        if (lane == 0) { lds[L_TAG] = (unsigned)T; lds[L_TAG + 1] = (unsigned)(T >> 32); LGKM0(); flg[F_QP] = 1u; }
        STAMP(1);
      }
    } else {
      const v4u z4 = {0u, 0u, 0u, 0u};
      __builtin_amdgcn_raw_buffer_store_b128(z4, rs, (int)(WS_PART + ((2 * g) * 32 + j) * 1024 + 16 * lane), 0, AUX_SC1);
      __builtin_amdgcn_raw_buffer_store_b128(z4, rs, (int)(WS_PART + ((2 * g + 1) * 32 + j) * 1024 + 16 * lane), 0, AUX_SC1);
      if (lane < 2) __builtin_amdgcn_raw_buffer_store_b64(v2u{0u, 0u}, rs, (int)(WS_ML + ((2 * g + lane) * 32 + j) * 8), 0, AUX_SC1);
      if (t2 && lane < 16) __builtin_amdgcn_raw_buffer_store_b128(z4, rs, (int)(WS_Y + (cb7 * 32 + hq * 2 + rh) * 256 + 16 * lane), 0, AUX_SC1);
      if (t3a) {
        if (lane < 32) __builtin_amdgcn_raw_buffer_store_b128(z4, rs, (int)(WS_CS + (cb * 7 + rb) * 512 + 16 * lane), 0, AUX_SC1);
        __builtin_amdgcn_raw_buffer_store_b128(z4, rs, (int)(WS_G + (cb * 7 + rb) * 1024 + 16 * lane), 0, AUX_SC1);
      }
      if (t3b) {
        __builtin_amdgcn_raw_buffer_store_b128(z4, rs, (int)(WS_F + fidx * 1792 + 16 * lane), 0, AUX_SC1);
        if (lane < 48) __builtin_amdgcn_raw_buffer_store_b128(z4, rs, (int)(WS_F + fidx * 1792 + 1024 + 16 * lane), 0, AUX_SC1);
      }
      if (lane == 0) __builtin_amdgcn_raw_buffer_store_b64(v2u{(unsigned)tstart, (unsigned)(tstart >> 32)}, rs, (int)(1024 + blockIdx.x * 8), 0, AUX_SC1);
      CBAR();
      if (lane == 0) LDS_INC(F_EI);
      VM0();
      const u64 T = (((u64)ev[1] << 32) | ev[0]) + 1ull;
      if (lane == 0) {
        __builtin_amdgcn_raw_buffer_store_b64(v2u{(unsigned)T, (unsigned)(T >> 32)}, rs, (int)(WS_READY + blockIdx.x * 8), 0, AUX_SC1);
        flg[F_T] = 1u;
      }
      STAMP(3);
    }
    __builtin_amdgcn_s_setprio(0);
    const u16* vb = vc + ((size_t)g * 8192 + 256 * j + 32 * v + (lane >> 4)) * 256 + 8 * (lane & 15);
    v4u V[16];
    while (flg[F_KI0 + v] == 0u) __builtin_amdgcn_s_sleep(1);
    CBAR();
    if (t2) wo_r = ldnt(wo_p);
    if (t3a) wgu_r = ldnt(wgu_p);
    if (t3b && v < 2) {
      const int tc = (tid - 512) < 112 ? (tid - 512) : 111;
#pragma unroll
      for (int r = 0; r < 8; ++r) wd_r[r] = __builtin_nontemporal_load((const v2u*)(wd + (size_t)(8 * fidx + r) * 448 + 4 * tc));
    }
    CBAR();
#pragma unroll
    for (int i = 0; i < 16; ++i) { V[i] = ldnt(vb + (i >> 1) * 1024 + (i & 1) * 128); CBAR(); }
    __builtin_amdgcn_s_setprio(2);
    if (t3a) {
      float a[8];
#pragma unroll
      for (int c = 0; c < 4; ++c) { a[2 * c] = bflo(wgu_r[c]); a[2 * c + 1] = bfhi(wgu_r[c]); }
      const float z = red8_b345(a, b3);
      ldf[L_CSP + wave * 64 + 8 * (lane & 7) + 4 * lb5 + 2 * lb4 + (lane >> 3 & 1)] = z;
      LGKM0();
      if (lane == 0) LDS_INC(F_CSC);
    }
    while (flg[F_P0 + v] == 0u) __builtin_amdgcn_s_sleep(2);
    CBAR();
    const LAS v4u* pp = (const LAS v4u*)(lds + L_P + v * 64 + ((lane >> 3) & 1) * 32 + (lane >> 4) * 8);
    const v4u pa = pp[0], pb = pp[1];
    const unsigned hm = (lane & 1) ? 0xffff0000u : 0x0000ffffu;
    const int rsel = (lane >> 1) & 3;
    const unsigned m0 = rsel == 0 ? hm : 0u, m1 = rsel == 1 ? hm : 0u, m2 = rsel == 2 ? hm : 0u, m3 = rsel == 3 ? hm : 0u;
    f32x4 a0 = {0.f, 0.f, 0.f, 0.f}, a1 = {0.f, 0.f, 0.f, 0.f};
#pragma unroll
    for (int kg = 0; kg < 8; ++kg) {
      const unsigned pd = kg < 4 ? pa[kg & 3] : pb[kg & 3];
      const v4u A = {pd & m0, pd & m1, pd & m2, pd & m3};
      a0 = mfma(A, V[2 * kg], a0);
      a1 = mfma(A, V[2 * kg + 1], a1);
    }
    LAS f32x4* rp = (LAS f32x4*)(ldf + L_RED + v * 512 + (lane >> 5) * 256 + 8 * (lane & 15) + 4 * ((lane >> 4) & 1));
    rp[0] = a0; rp[32] = a1;
    LGKM0();
    unsigned oldv = 0;
    if (lane == 0) oldv = LDS_INC(F_VC);
    oldv = __builtin_amdgcn_readfirstlane(oldv);
    if (oldv == 7u) {
      CBAR();
      STAMP(6);
      const LAS f32x4* sp = (const LAS f32x4*)(ldf + L_RED + 4 * lane);
      f32x4 r0[8], r1[8];
#pragma unroll
      for (int w = 0; w < 8; ++w) { r0[w] = sp[w * 128]; r1[w] = sp[w * 128 + 64]; }
      const f32x4 s0 = ((r0[0] + r0[1]) + (r0[2] + r0[3])) + ((r0[4] + r0[5]) + (r0[6] + r0[7]));
      const f32x4 s1 = ((r1[0] + r1[1]) + (r1[2] + r1[3])) + ((r1[4] + r1[5]) + (r1[6] + r1[7]));
      v4u e0 = {enc(s0[0]), enc(s0[1]), enc(s0[2]), enc(s0[3])};
      v4u e1 = {enc(s1[0]), enc(s1[1]), enc(s1[2]), enc(s1[3])};
      __builtin_amdgcn_raw_buffer_store_b128(e0, rs, (int)(WS_PART + ((2 * g) * 32 + j) * 1024 + 16 * lane), 0, AUX_SC1);
      __builtin_amdgcn_raw_buffer_store_b128(e1, rs, (int)(WS_PART + ((2 * g + 1) * 32 + j) * 1024 + 16 * lane), 0, AUX_SC1);
      STAMP(7);
    }
  }
  if (t2) {
    {
      while (flg[F_VC] < 8u || flg[F_R] == 0u) __builtin_amdgcn_s_sleep(1);
      CBAR();
      v4u pa;
      unsigned spins = 0;
      const int off = (int)(WS_PART + (hq * 32 + (lane >> 1)) * 1024 + (128 * rh + 8 * wave + 4 * (lane & 1)) * 4);
      bool ok = false;
      __builtin_amdgcn_s_sleep(T2DELAY);
      for (;;) {
        CBAR();
        if (!ok) {
          pa = __builtin_amdgcn_raw_buffer_load_b128(rs, off, 0, AUX_SC1);
          ok = (pa[0] != 0u) & (pa[1] != 0u) & (pa[2] != 0u) & (pa[3] != 0u);
        }
        if (__all(ok) || ++spins > SPIN_CAP) break;
        __builtin_amdgcn_s_sleep(PSLEEP);
      }
      if (wave == 0) STAMP(8);
      if (wave == 15) STAMP(21);
      if (wave == 7) STAMP(22);
      while (flg[F_W2] == 0u) __builtin_amdgcn_s_sleep(1);
      CBAR();
      const float w = ldf[L_WTS + (lane >> 1)];
      const float u0 = tr32(w * dec(pa[0]), w * dec(pa[2])), u1 = tr32(w * dec(pa[1]), w * dec(pa[3]));
      float o = tr16(u0, u1);
      o += dpp<XOR2>(o); o += dpp<ROR8>(o); o += dpp<ROR4>(o);
      o = rbf(o);
      float y[8];
#pragma unroll
      for (int c = 0; c < 4; ++c) { y[2 * c] = o * bflo(wo_r[c]); y[2 * c + 1] = o * bfhi(wo_r[c]); }
#pragma unroll
      for (int c = 0; c < 4; ++c) y[c] = tr32(y[c], y[c + 4]);
#pragma unroll
      for (int c = 0; c < 2; ++c) y[c] = tr16(y[c], y[c + 2]);
      const float yy = trd<XOR1>(y[0], y[1], b0);
      ldf[L_RED2 + wave * 64 + 8 * ((lane >> 1) & 7) + 4 * lb5 + 2 * lb4 + (lane & 1)] = yy;
    }
    __syncthreads();
    if (wave == 0) {
      STAMP(18);
      const LAS f32x4* rp = (const LAS f32x4*)(ldf + L_RED2 + (lane >> 4) * 256 + 4 * (lane & 15));
      const f32x4 a = (rp[0] + rp[16]) + (rp[32] + rp[48]);
      const float u0 = tr32(a[0], a[2]), u1 = tr32(a[1], a[3]);
      const float z = tr16(u0, u1);
      __builtin_amdgcn_raw_buffer_store_b32(enc(z), rs, (int)(WS_Y + (cb7 * 32 + hq * 2 + rh) * 256 + (4 * (lane & 15) + 2 * lb5 + lb4) * 4), 0, AUX_SC1);
      STAMP(9);
    }
  }
  if (t3a) {
    const unsigned xraw = (unsigned)x[64 * rb + 8 * (wave & 7) + (lane >> 3)];
    if (wave >= 8) {
      v4u yv;
      unsigned spins = 0;
      bool ok = false;
      const int off = (int)(WS_Y + (rb * 32 + 4 * (wave - 8) + (lane >> 4)) * 256 + (lane & 15) * 16);
      __builtin_amdgcn_s_sleep(T3DELAY);
      for (;;) {
        CBAR();
        if (!ok) {
          yv = __builtin_amdgcn_raw_buffer_load_b128(rs, off, 0, AUX_SC1);
          ok = (yv[0] != 0u) & (yv[1] != 0u) & (yv[2] != 0u) & (yv[3] != 0u);
        }
        if (__all(ok) || ++spins > SPIN_CAP) break;
        __builtin_amdgcn_s_sleep(PSLEEP);
      }
      if (wave == 8) STAMP(10);
      if (wave == 15) STAMP(19);
      if (wave == 12) STAMP(20);
      const float u0 = tr32(dec(yv[0]), dec(yv[2])), u1 = tr32(dec(yv[1]), dec(yv[3]));
      ldf[L_HP + (wave - 8) * 64 + 4 * (lane & 15) + 2 * lb5 + lb4] = tr16(u0, u1);
    }
    __syncthreads();
    if (wave == 0) STAMP(14);
    {
      const int row = 8 * (wave & 7) + (lane >> 3);
      float t8[8];
#pragma unroll
      for (int w = 0; w < 8; ++w) t8[w] = ldf[L_HP + w * 64 + row];
      const float at = ((t8[0] + t8[1]) + (t8[2] + t8[3])) + ((t8[4] + t8[5]) + (t8[6] + t8[7]));
      const float h = rbf(bflo(xraw) + rbf(at));
      float a[8];
#pragma unroll
      for (int c = 0; c < 4; ++c) { a[2 * c] = h * bflo(wgu_r[c]); a[2 * c + 1] = h * bfhi(wgu_r[c]); }
      const float z = red8_b345(a, b3);
      ldf[L_RED3 + wave * 64 + 8 * (lane & 7) + 4 * lb5 + 2 * lb4 + (lane >> 3 & 1)] = z;
      if (wave < 8 && (lane & 7) == 0) ldf[L_QP + row] = h;
    }
    __syncthreads();
    if (wave == 0) STAMP(15);
    if (wave < 2) {
      float t8[8];
#pragma unroll
      for (int w = 0; w < 8; ++w) t8[w] = ldf[L_RED3 + (wave * 8 + w) * 64 + lane];
      const float z = ((t8[0] + t8[1]) + (t8[2] + t8[3])) + ((t8[4] + t8[5]) + (t8[6] + t8[7]));
      __builtin_amdgcn_raw_buffer_store_b32(enc(z), rs, (int)(WS_G + (cb * 7 + rb) * 1024 + (lane >> 3) * 128 + (8 * wave + (lane & 7)) * 4), 0, AUX_SC1);
    } else if (wave == 2) {
      const float h = ldf[L_QP + lane];
      const float piv = asf((unsigned)__builtin_amdgcn_readfirstlane((int)asu(h)));
      const float d = h - piv;
      float st = tr32(d, d * d);
      st = tr16(st, st); st += dpp<XOR1>(st); st += dpp<XOR2>(st); st += dpp<ROR8>(st); st += dpp<ROR4>(st);
      const int k = lane >> 3;
      if (k == 0 || k == 1 || k == 4) {
        const float val = k == 0 ? piv : st;
        __builtin_amdgcn_raw_buffer_store_b32(enc(val), rs, (int)(WS_G + (cb * 7 + rb) * 1024 + (lane & 7) * 128 + (16 + (k == 4 ? 2 : k)) * 4), 0, AUX_SC1);
      }
    }
    if (wave == 0) STAMP(16);
  }
  if (t3b) {
    if (wave == 8 || wave == 9) {
      if (!t3a) { while (flg[F_VC] < 8u || flg[F_R] == 0u) __builtin_amdgcn_s_sleep(1); __builtin_amdgcn_s_sleep(40); }
      v4u gv = {1u, 1u, 1u, 1u};
      bool ok = (lane >= 56) || ((lane & 7) > 4);
      unsigned spins = 0;
      const int off = (int)(WS_G + ((fidx >> 3) * 7 + (lane >> 3)) * 1024 + (fidx & 7) * 128 + (lane & 7) * 16);
      __builtin_amdgcn_s_sleep(T3BDELAY);
      for (;;) {
        CBAR();
        if (!ok) {
          gv = __builtin_amdgcn_raw_buffer_load_b128(rs, off, 0, AUX_SC1);
          ok = (gv[0] != 0u) & (gv[1] != 0u) & (gv[2] != 0u) & ((gv[3] != 0u) | ((lane & 7) == 4));
        }
        if (__all(ok) || ++spins > SPIN_CAP) break;
        __builtin_amdgcn_s_sleep(PSLEEP);
      }
      if (wave == 8) STAMP(17);
      while (flg[F_CS] == 0u) __builtin_amdgcn_s_sleep(1);
      CBAR();
      const bool live = lane < 56;
      const bool isst = (lane & 7) == 4;
      const float piv = dec(gv[0]), s1 = dec(gv[1]), s2 = dec(gv[2]);
      const float dm = s1 * (1.f / 64.f);
      const float mw = piv + dm, m2 = s2 - s1 * dm;
      float a0 = live ? (isst ? mw : dec(gv[0])) : 0.f, a1 = live ? dec(gv[1]) : 0.f, a2 = live ? dec(gv[2]) : 0.f, a3 = live ? dec(gv[3]) : 0.f;
      a0 = tr32(a0, a0); a1 = tr32(a1, a1); a2 = tr32(a2, a2); a3 = tr32(a3, a3);
      a0 = tr16(a0, a0); a1 = tr16(a1, a1); a2 = tr16(a2, a2); a3 = tr16(a3, a3);
      a0 += dpp<ROR8>(a0); a1 += dpp<ROR8>(a1); a2 += dpp<ROR8>(a2); a3 += dpp<ROR8>(a3);
      const float mean = asf((unsigned)__builtin_amdgcn_readlane((int)asu(a0), 4)) * (1.f / 7.f);
      const float dd = mw - mean;
      float vv = (live && isst) ? m2 + 64.f * dd * dd : 0.f;
      vv = tr32(vv, vv); vv = tr16(vv, vv); vv += dpp<ROR8>(vv);
      const float var = fmaxf(asf((unsigned)__builtin_amdgcn_readlane((int)asu(vv), 4)) * (1.f / 448.f), 0.f);
      const float mb = rbf(mean), den = rbf(__builtin_sqrtf(rbf(rbf(var) + 1e-5f)));
      const float rden = 1.f / den;
      const f32x4 cs4 = *(const LAS f32x4*)(ldf + L_CST + 4 * (lane & 3));
      const float z0 = rbf((a0 - mb * cs4[0]) * rden), z1 = rbf((a1 - mb * cs4[1]) * rden), z2 = rbf((a2 - mb * cs4[2]) * rden), z3 = rbf((a3 - mb * cs4[3]) * rden);
      const float u0 = dpp<XOR2>(z0), u1 = dpp<XOR2>(z1), u2 = dpp<XOR2>(z2), u3 = dpp<XOR2>(z3);
      float av[4];
      {
        const float zz[4] = {z0, z1, z2, z3}, uu[4] = {u0, u1, u2, u3};
#pragma unroll
        for (int i = 0; i < 4; ++i) {
          const float z = zz[i];
          const float t = 0.7978845608028654f * (z + 0.044715f * z * z * z);
          const float e2 = __builtin_amdgcn_exp2f(t * 2.8853900817779268f);
          const float th = 1.f - 2.f * __builtin_amdgcn_rcpf(e2 + 1.f);
          av[i] = rbf(rbf(0.5f * z * (1.f + th)) * uu[i]);
        }
      }
      f32x4 f = {0.f, 0.f, 0.f, 0.f};
#pragma unroll
      for (int r = 0; r < 8; ++r) {
        const float ar = asf((unsigned)__builtin_amdgcn_readlane((int)asu(av[r & 3]), r >> 2));
        f[0] = fmaf(ar, bflo(wd_r[r][0]), f[0]); f[1] = fmaf(ar, bfhi(wd_r[r][0]), f[1]);
        f[2] = fmaf(ar, bflo(wd_r[r][1]), f[2]); f[3] = fmaf(ar, bfhi(wd_r[r][1]), f[3]);
      }
      if (tid - 512 < 112) {
        v4u e = {enc(f[0]), enc(f[1]), enc(f[2]), enc(f[3])};
        __builtin_amdgcn_raw_buffer_store_b128(e, rs, (int)(WS_F + fidx * 1792 + 16 * (tid - 512)), 0, AUX_SC1);
      }
      if (wave == 8) STAMP(11);
    }
  } else if (t4) {
    const int rid = j;
    {
      const int off = wave < 14 ? (int)(WS_F + (16 * wave + (lane >> 2)) * 1792 + (16 * rid + 4 * (lane & 3)) * 4)
                                : (int)(WS_Y + ((rid >> 2) * 32 + 16 * (wave - 14) + (lane >> 2)) * 256 + (16 * (rid & 3) + 4 * (lane & 3)) * 4);
      v4u pv;
      unsigned spins = 0;
      bool ok = false;
      if (wave < 14) { while (flg[F_Y4] < 2u) __builtin_amdgcn_s_sleep(1); __builtin_amdgcn_s_sleep(T4DELAY); }
      else __builtin_amdgcn_s_sleep(T3DELAY);
      for (;;) {
        CBAR();
        if (!ok) {
          pv = __builtin_amdgcn_raw_buffer_load_b128(rs, off, 0, AUX_SC1);
          ok = (pv[0] != 0u) & (pv[1] != 0u) & (pv[2] != 0u) & (pv[3] != 0u);
        }
        if (__all(ok) || ++spins > SPIN_CAP) break;
        __builtin_amdgcn_s_sleep(PSLEEP);
      }
      if (wave >= 14 && lane == 0) LDS_INC(F_Y4);
      if (wave == 0) STAMP(12);
      const float a0 = tr32(dec(pv[0]), dec(pv[2])), a1 = tr32(dec(pv[1]), dec(pv[3]));
      float z = tr16(a0, a1);
      z += dpp<ROR8>(z); z += dpp<ROR4>(z);
      const int col = 4 * (lane & 3) + 2 * lb5 + lb4;
      if ((lane & 12) == 0) ldf[(wave < 14 ? L_RED4 + wave * 16 : L_H4 + (wave - 14) * 16) + col] = z;
    }
    const unsigned xraw = (unsigned)x[16 * rid + (tid & 15)];
    __syncthreads();
    if (tid < 16) {
      float t[14];
#pragma unroll
      for (int w = 0; w < 14; ++w) t[w] = ldf[L_RED4 + w * 16 + tid];
      const float f = (((t[0] + t[1]) + (t[2] + t[3])) + ((t[4] + t[5]) + (t[6] + t[7]))) + (((t[8] + t[9]) + (t[10] + t[11])) + (t[12] + t[13]));
      const float h = rbf(bflo(xraw) + rbf(ldf[L_H4 + tid] + ldf[L_H4 + 16 + tid]));
      out[16 * rid + tid] = (u16)f2bf(h + rbf(f));
    }
    if (wave == 0) STAMP(13);
  }
  __syncthreads();
  if (wave == 0) {
    u64 mn = ~0ull;
    for (int k = 0; k < 4; ++k) { const v2u r = __builtin_amdgcn_raw_buffer_load_b64(rs, (int)(1024 + (lane + 64 * k) * 8), 0, AUX_SC1); const u64 t = ((u64)r[1] << 32) | r[0]; mn = t < mn ? t : mn; }
    for (int o = 32; o >= 1; o >>= 1) { const u64 t = __shfl_xor(mn, o); mn = t < mn ? t : mn; }
    while (__builtin_amdgcn_s_memrealtime() - mn < 5000ull) __builtin_amdgcn_s_sleep(1);
  }
}

extern "C" void kernel_launch(void* const* d_in, const int* in_sizes, int n_in,
                              void* d_out, int out_size, void* d_ws, size_t ws_size,
                              hipStream_t stream) {
  fused_decode<<<256, 1024, 0, stream>>>((const u16*)d_in[0], (const u16*)d_in[1], (const u16*)d_in[2], (const u16*)d_in[3],
                                         (const u16*)d_in[4], (const u16*)d_in[6], (const u16*)d_in[7], (const u16*)d_in[8],
                                         (u16*)d_out, (unsigned char*)d_ws);
}
```

```cpp
#include <hip/hip_runtime.h>
#include <stdint.h>

typedef unsigned short u16;
typedef unsigned long long u64;
typedef unsigned v4u __attribute__((ext_vector_type(4)));
typedef unsigned v2u __attribute__((ext_vector_type(2)));
typedef float f32x4 __attribute__((ext_vector_type(4)));
typedef short bf16x8 __attribute__((ext_vector_type(8)));
#define LAS __attribute__((address_space(3)))

constexpr int AUX_SC1 = 16;
constexpr unsigned EMPTY = 0xffffffffu;
constexpr unsigned WS_E = 0;
constexpr unsigned WS_READY = 4096;
constexpr unsigned WS_QG = 8192;
constexpr unsigned WS_ML = 139264;
constexpr unsigned WS_Y = 143360;
constexpr unsigned WS_ST = 200704;
constexpr unsigned WS_G = 204800;
constexpr unsigned WS_PART = 405504;
constexpr unsigned WS_F = 929792;
constexpr unsigned WS_END = 1331200;
constexpr unsigned SPIN_CAP = 1u << 17;
constexpr int KPRE = 2;
constexpr int QDELAY = 16, T2DELAY = 6, T3DELAY = 4, T3BDELAY = 24, T4DELAY = 64;
constexpr int PSLEEP = 1, P2SLEEP = 1, P3BSLEEP = 1, P4SLEEP = 1;
constexpr unsigned MLWAIT = 8u;
constexpr int CODEBYTES = 13824;

__device__ __forceinline__ float asf(unsigned u) { return __builtin_bit_cast(float, u); }
__device__ __forceinline__ unsigned asu(float f) { return __builtin_bit_cast(unsigned, f); }
__device__ __forceinline__ float bflo(unsigned w) { return asf(w << 16); }
__device__ __forceinline__ float bfhi(unsigned w) { return asf(w & 0xffff0000u); }
__device__ __forceinline__ unsigned f2bf(float f) { __bf16 b = (__bf16)f; return (unsigned)__builtin_bit_cast(u16, b); }
__device__ __forceinline__ float rbf(float f) { return asf(f2bf(f) << 16); }
__device__ __forceinline__ unsigned bfdup(float f) { const unsigned b = f2bf(f); return b | (b << 16); }
__device__ __forceinline__ bool full4(v4u v) { return (v[0] != EMPTY) & (v[1] != EMPTY) & (v[2] != EMPTY) & (v[3] != EMPTY); }

template <int CTRL> __device__ __forceinline__ unsigned dppu(unsigned x) {
  return (unsigned)__builtin_amdgcn_mov_dpp((int)x, CTRL, 0xf, 0xf, true);
}
template <int CTRL> __device__ __forceinline__ float dpp(float x) { return asf(dppu<CTRL>(asu(x))); }
constexpr int XOR1 = 0xB1, XOR2 = 0x4E, ROR4 = 0x124, ROR8 = 0x128, HMIR = 0x141, BCAST0 = 0x150;
__device__ __forceinline__ float tr32(float a, float b) {
  auto r = __builtin_amdgcn_permlane32_swap(asu(a), asu(b), false, false);
  return asf(r[0]) + asf(r[1]);
}
__device__ __forceinline__ float tr16(float a, float b) {
  auto r = __builtin_amdgcn_permlane16_swap(asu(a), asu(b), false, false);
  return asf(r[0]) + asf(r[1]);
}
template <int CTRL> __device__ __forceinline__ float trd(float a, float b, bool bit) {
  const float t = bit ? a : b, k = bit ? b : a;
  return k + dpp<CTRL>(t);
}
__device__ __forceinline__ float mx32(float a) {
  auto r = __builtin_amdgcn_permlane32_swap(asu(a), asu(a), false, false);
  return fmaxf(asf(r[0]), asf(r[1]));
}
__device__ __forceinline__ float mx16(float a) {
  auto r = __builtin_amdgcn_permlane16_swap(asu(a), asu(a), false, false);
  return fmaxf(asf(r[0]), asf(r[1]));
}
__device__ __forceinline__ float wave_sum(float x) {
  x += dpp<XOR1>(x); x += dpp<XOR2>(x); x += dpp<ROR8>(x); x += dpp<ROR4>(x);
  x = tr16(x, x); x = tr32(x, x);
  return x;
}
__device__ __forceinline__ float wave_max(float x) {
  x = fmaxf(x, dpp<XOR1>(x)); x = fmaxf(x, dpp<XOR2>(x)); x = fmaxf(x, dpp<ROR8>(x)); x = fmaxf(x, dpp<ROR4>(x));
  x = mx16(x); x = mx32(x);
  return x;
}
__device__ __forceinline__ float red8_b345(float (&a)[8], bool b3) {
#pragma unroll
  for (int c = 0; c < 4; ++c) a[c] = tr32(a[c], a[c + 4]);
#pragma unroll
  for (int c = 0; c < 2; ++c) a[c] = tr16(a[c], a[c + 2]);
  return trd<ROR8>(a[0], a[1], b3);
}
__device__ __forceinline__ v4u ldnt(const u16* p) { return __builtin_nontemporal_load((const v4u*)p); }
__device__ __forceinline__ f32x4 mfma(v4u a, v4u b, f32x4 c) {
  return __builtin_amdgcn_mfma_f32_16x16x32_bf16(__builtin_bit_cast(bf16x8, a), __builtin_bit_cast(bf16x8, b), c, 0, 0, 0);
}
#define OPAQUE(o) asm volatile("" : "+v"(o) :: "memory")
__device__ __forceinline__ v4u ld128(__amdgpu_buffer_rsrc_t rs, int off, int soff = 0) { return __builtin_amdgcn_raw_buffer_load_b128(rs, off, soff, AUX_SC1); }
__device__ __forceinline__ v2u ld64(__amdgpu_buffer_rsrc_t rs, int off) { return __builtin_amdgcn_raw_buffer_load_b64(rs, off, 0, AUX_SC1); }
#define LGKM0() asm volatile("s_waitcnt lgkmcnt(0)" ::: "memory")
#define VM0() asm volatile("s_waitcnt vmcnt(0)" ::: "memory")
#define CBAR() asm volatile("" ::: "memory")
#define LDS_INC(f) __hip_atomic_fetch_add((LAS unsigned*)(lds + L_FLAG + (f)), 1u, __ATOMIC_RELAXED, __HIP_MEMORY_SCOPE_WORKGROUP)

constexpr int L_Q = 0;
constexpr int L_QP = 256;
constexpr int L_P = 704;
constexpr int L_MW = 1216;
constexpr int L_LW = 1232;
constexpr int L_WTS = 1248;
constexpr int L_FLAG = 1280;
constexpr int L_TAG = 1312;
constexpr int L_RED4 = 1328;
constexpr int L_AV = 1552;
constexpr int L_RED2 = 1600;
constexpr int L_GU = 3648;
constexpr int L_RED = 7744;
constexpr int L_RED1 = L_RED + 4096;
constexpr int L_TOTAL = L_RED1 + 2112;
constexpr int F_QD = 0, F_R = 1, F_MC = 2, F_LC = 3, F_VC = 4, F_T = 5, F_QC = 6, F_P0 = 8, F_KI0 = 16,
              F_Y4 = 24, F_W2 = 25, F_QP = 26, F_GU = 27, F_YP = 28, F_GP = 29, F_AV = 30;

__global__ void __launch_bounds__(1024, 4) fused_decode(const u16* __restrict__ x, const u16* __restrict__ kc, const u16* __restrict__ vc,
                                                     const u16* __restrict__ wq, const u16* __restrict__ wo, const u16* __restrict__ wg,
                                                     const u16* __restrict__ wu, const u16* __restrict__ wd, u16* __restrict__ out,
                                                     unsigned char* ws) {
  __shared__ __attribute__((aligned(16))) unsigned lds_raw[L_TOTAL];
  LAS unsigned* lds = (LAS unsigned*)lds_raw;
  LAS float* ldf = (LAS float*)lds_raw;
  volatile LAS unsigned* flg = (volatile LAS unsigned*)(lds + L_FLAG);
  const int tid = threadIdx.x, lane = tid & 63, wave = __builtin_amdgcn_readfirstlane(tid >> 6);
  unsigned cpf = 0u;
  if (wave < 2) {
    const u64 pc = (u64)__builtin_amdgcn_s_getpc();
    const int o = (wave * 64 + lane) * 128;
    if (o < CODEBYTES) cpf = *(const __attribute__((address_space(1))) unsigned*)(pc + o);
  }
  asm volatile("" :: "s"(x), "s"(kc), "s"(vc), "s"(wq), "s"(wo), "s"(wg), "s"(wu), "s"(wd), "s"(out), "s"(ws));
  const int g = blockIdx.x & 7, j = blockIdx.x >> 3;
  const bool t2 = j < 28;
  const int jj = t2 ? j : 0;
  const int hq = 2 * g + jj / 14, rh = (jj % 14) / 7, cb7 = jj % 7;
  const bool t3a = (g < 7) && t2;
  const int cb = 4 * g + jj / 7, rb = jj % 7;
  const bool t3b = (g < 7) != t2;
  const int fu = g < 7 ? 8 * g + 2 * (j - 28) : 8 * (jj >> 2) + 2 * (jj & 3) + 1;
  const bool t4 = (g == 7) && t2;
  const __amdgpu_buffer_rsrc_t rs = __builtin_amdgcn_make_buffer_rsrc((void*)ws, 0, (int)WS_END, 0x00020000);
  if (tid < 32) lds[L_FLAG + tid] = 0u;

  const bool b0 = lane & 1, b1 = lane & 2, b3 = lane & 8;
  const int lb3 = (lane >> 3) & 1, lb4 = (lane >> 4) & 1, lb5 = (lane >> 5) & 1;
  const unsigned hm = (lane & 1) ? 0xffff0000u : 0x0000ffffu;
  const int rsel = (lane >> 1) & 3;
  const unsigned m0 = rsel == 0 ? hm : 0u, m1 = rsel == 1 ? hm : 0u, m2 = rsel == 2 ? hm : 0u, m3 = rsel == 3 ? hm : 0u;
  v4u wo_r, wgu_r;
  const u16* wo_p = wo + (size_t)(hq * 256 + 128 * rh + 8 * wave + 4 * lb3 + (lane >> 4)) * 448 + 64 * cb7 + 8 * (lane & 7);
  const u16* wgu_p = (b3 ? wu : wg) + (size_t)(64 * rb + 4 * wave + (lane >> 4)) * 1792 + 64 * cb + 8 * (lane & 7);

  auto do_t2 = [&]() __attribute__((always_inline)) {
  if (t2) {
    {
      while (flg[F_VC] < 8u) __builtin_amdgcn_s_sleep(1);
      CBAR();
      v4u pa;
      unsigned spins = 0;
      const int sp = 2 * wave + lb5;
      int off = (int)(WS_PART + (hq * 32 + sp) * 1024 + (128 * rh + 4 * (lane & 31)) * 4);
      bool ok = false;
      __builtin_amdgcn_s_sleep(T2DELAY);
      for (;;) {
        OPAQUE(off);
        if (!ok) { pa = ld128(rs, off); ok = full4(pa); }
        if (__all(ok) || ++spins > SPIN_CAP) break;
        __builtin_amdgcn_s_sleep(P2SLEEP);
      }
      while (flg[F_W2] == 0u) __builtin_amdgcn_s_sleep(1);
      CBAR();
      const float w = ldf[L_WTS + sp];
      const float u0 = tr32(w * asf(pa[0]), w * asf(pa[2])), u1 = tr32(w * asf(pa[1]), w * asf(pa[3]));
      *(LAS v2u*)(lds + L_RED1 + wave * 132 + 4 * (lane & 31) + 2 * lb5) = v2u{asu(u0), asu(u1)};
      __syncthreads();
      const int row = 8 * wave + 4 * lb3 + (lane >> 4);
      float o = ldf[L_RED1 + (2 * (lane & 7)) * 132 + row] + ldf[L_RED1 + (2 * (lane & 7) + 1) * 132 + row];
      o += dpp<XOR1>(o); o += dpp<XOR2>(o); o += dpp<HMIR>(o);
      const unsigned od = bfdup(o);
      const v4u A = {od & m0, od & m1, od & m2, od & m3};
      const f32x4 zero = {0.f, 0.f, 0.f, 0.f};
      const f32x4 y = mfma(A, wo_r, zero);
      if (lb5 == lb3) *(LAS f32x4*)(ldf + L_RED2 + (2 * wave + lb5) * 64 + 8 * (lane & 7) + 4 * lb4) = y;
    }
    __syncthreads();
    if (wave == 0) {
      const LAS f32x4* rp = (const LAS f32x4*)(ldf + L_RED2 + (lane >> 4) * 512 + 4 * (lane & 15));
      f32x4 r[8];
#pragma unroll
      for (int i = 0; i < 8; ++i) r[i] = rp[16 * i];
      f32x4 a = ((r[0] + r[1]) + (r[2] + r[3])) + ((r[4] + r[5]) + (r[6] + r[7]));
#pragma unroll
      for (int c = 0; c < 4; ++c) { a[c] = tr32(a[c], a[c]); a[c] = tr16(a[c], a[c]); }
      if (lane < 16) __builtin_amdgcn_raw_buffer_store_b128(__builtin_bit_cast(v4u, a), rs, (int)(WS_Y + (cb7 * 32 + hq * 2 + rh) * 256 + 16 * lane), 0, AUX_SC1);
    }
  }
  };
  if (__builtin_expect(wave < 8, 1)) {
    const u16* kb = kc + ((size_t)g * 8192 + 256 * j + 32 * wave + ((lane >> 2) & 3)) * 256 + 8 * ((lane & 3) + 4 * (lane >> 4));
    v4u K[16];
#pragma unroll
    for (int i = 0; i < KPRE; ++i) { K[i] = ldnt(kb + (i >> 1) * 1024 + (i & 1) * 128); CBAR(); }
    __syncthreads();
#pragma unroll
    for (int i = KPRE; i < 16; ++i) { K[i] = ldnt(kb + (i >> 1) * 1024 + (i & 1) * 128); CBAR(); }
    if (t2) wo_r = ldnt(wo_p);
    if (t3a) wgu_r = ldnt(wgu_p);
    CBAR();
    if (lane == 0) flg[F_KI0 + wave] = 1u;
    {
      while (flg[F_QP] == 0u) __builtin_amdgcn_s_sleep(1);
      CBAR();
      const unsigned tl = lds[L_TAG] & 0xfu, th = (lds[L_TAG] >> 4) | (lds[L_TAG + 1] << 28);
      __builtin_amdgcn_s_setprio(3);
      v4u qa, qb;
      unsigned spins = 0;
      bool ok = false;
      int off = (int)(WS_QG + (8 * g + wave) * 2048 + (lane >> 5) * 512 + (lane & 31) * 16);
      __builtin_amdgcn_s_sleep(QDELAY);
      for (;;) {
        OPAQUE(off);
        if (!ok) {
          qa = ld128(rs, off);
          qb = ld128(rs, off, 1024);
          ok = ((qa[0] & 0xfu) == tl) & (qa[1] == th) & ((qa[2] & 0xfu) == tl) & (qa[3] == th) &
               ((qb[0] & 0xfu) == tl) & (qb[1] == th) & ((qb[2] & 0xfu) == tl) & (qb[3] == th);
        }
        if (__all(ok) || ++spins > SPIN_CAP) break;
        __builtin_amdgcn_s_sleep(8);
      }
      const float c0 = asf(qa[0] & 0xfffffff0u) + asf(qb[0] & 0xfffffff0u), c1 = asf(qa[2] & 0xfffffff0u) + asf(qb[2] & 0xfffffff0u);
      const float s0 = tr32(c0, c0), s1 = tr32(c1, c1);
      if (lane < 32) lds[L_Q + 32 * wave + lane] = f2bf(s0) | (f2bf(s1) << 16);
      LGKM0();
      if (lane == 0) LDS_INC(F_QD);
      __builtin_amdgcn_s_setprio(0);
    }
    while (flg[F_QD] < 8u) __builtin_amdgcn_s_sleep(1);
    CBAR();
    v4u qf[2];
    {
      const int head = (lane >> 2) & 1;
      const LAS v4u* qp = (const LAS v4u*)(lds + L_Q + head * 128 + 4 * ((lane & 3) + 4 * (lane >> 4)));
      qf[0] = qp[0]; qf[1] = qp[16];
    }
    const int mykg = (lane & 3) | (lb3 << 2);
    float s = 0.f;
#pragma unroll
    for (int kg = 0; kg < 8; ++kg) {
      f32x4 acc = {0.f, 0.f, 0.f, 0.f};
      acc = mfma(K[2 * kg], qf[0], acc);
      acc = mfma(K[2 * kg + 1], qf[1], acc);
      float v = b1 ? (b0 ? acc[3] : acc[2]) : (b0 ? acc[1] : acc[0]);
      v += dpp<XOR1>(v); v += dpp<XOR2>(v);
      s = (mykg == kg) ? v : s;
    }
    s = rbf(s) * 0.0625f;
    float m = s;
    m = fmaxf(m, dpp<XOR1>(m)); m = fmaxf(m, dpp<XOR2>(m)); m = fmaxf(m, dpp<ROR8>(m)); m = mx16(m); m = mx32(m);
    if ((lane & ~4) == 0) ldf[L_MW + wave * 2 + (lane >> 2)] = m;
    LGKM0();
    if (lane == 0) LDS_INC(F_MC);
    while (flg[F_MC] < 8u) __builtin_amdgcn_s_sleep(1);
    CBAR();
    {
      const int head = (lane >> 2) & 1;
      float mm = ldf[L_MW + head];
#pragma unroll
      for (int w = 1; w < 8; ++w) mm = fmaxf(mm, ldf[L_MW + 2 * w + head]);
      m = mm;
    }
    const float p = __builtin_amdgcn_exp2f((s - m) * 1.4426950408889634f);
    const unsigned pb = f2bf(p);
    lds[L_P + wave * 64 + ((lane >> 2) & 1) * 32 + (lane >> 4) * 8 + mykg] = pb | (pb << 16);
    float l = asf(pb << 16);
    l += dpp<XOR1>(l); l += dpp<XOR2>(l); l += dpp<ROR8>(l); l = tr16(l, l); l = tr32(l, l);
    if ((lane & ~4) == 0) ldf[L_LW + wave * 2 + (lane >> 2)] = l;
    LGKM0();
    unsigned old = 0;
    if (lane == 0) { flg[F_P0 + wave] = 1u; old = LDS_INC(F_LC); }
    old = __builtin_amdgcn_readfirstlane(old);
    if (old == 7u) {
      while (flg[F_T] == 0u) __builtin_amdgcn_s_sleep(1);
      CBAR();
      __builtin_amdgcn_s_setprio(3);
      if ((lane & ~4) == 0) {
        const int head = lane >> 2;
        float ls = 0.f;
#pragma unroll
        for (int w = 0; w < 8; ++w) ls += ldf[L_LW + 2 * w + head];
        v2u rec = {asu(m), asu(ls)};
        __builtin_amdgcn_raw_buffer_store_b64(rec, rs, (int)(WS_ML + ((2 * g + head) * 32 + j) * 8), 0, AUX_SC1);
      }
      __builtin_amdgcn_s_setprio(0);
    }
    asm volatile("" :: "v"(cpf));
    if (t3a) {
      *(LAS v4u*)(lds + L_GU + wave * 256 + lane * 4) = wgu_r;
      LGKM0();
      if (lane == 0) LDS_INC(F_GU);
    }
    if (wave == 6) {
      CBAR();
      const unsigned t0 = lds[L_TAG], t1 = lds[L_TAG + 1];
      unsigned spins = 0;
      int off = (int)(WS_READY + lane * 16);
      for (;;) {
        OPAQUE(off);
        const v4u r0 = ld128(rs, off);
        const v4u r1 = ld128(rs, off, 1024);
        const bool ok = (r0[0] == t0) & (r0[1] == t1) & (r0[2] == t0) & (r0[3] == t1) & (r1[0] == t0) & (r1[1] == t1) & (r1[2] == t0) & (r1[3] == t1);
        if (__all(ok) || ++spins > SPIN_CAP) break;
        __builtin_amdgcn_s_sleep(8);
      }
      if (lane == 0) {
        flg[F_R] = 1u;
        if (blockIdx.x == 0) __builtin_amdgcn_raw_buffer_store_b64(v2u{t0, t1}, rs, (int)WS_E, 0, AUX_SC1);
      }
    }
    if (wave == 7) {
      while (flg[F_VC] < MLWAIT) __builtin_amdgcn_s_sleep(1);
      __builtin_amdgcn_s_setprio(3);
      if (t2) {
        v2u rec = {0u, 0u};
        bool ok = lane >= 32;
        unsigned spins = 0;
        int off = (int)(WS_ML + (hq * 32 + lane) * 8);
        for (;;) {
          OPAQUE(off);
          if (!ok) { rec = ld64(rs, off); ok = (rec[0] != EMPTY) & (rec[1] != EMPTY); }
          if (__all(ok) || ++spins > SPIN_CAP) break;
          __builtin_amdgcn_s_sleep(PSLEEP);
        }
        const float ms = lane < 32 ? asf(rec[0]) : -INFINITY, lsum = lane < 32 ? asf(rec[1]) : 0.f;
        const float M = wave_max(ms);
        const float e = lane < 32 ? __builtin_amdgcn_exp2f((ms - M) * 1.4426950408889634f) : 0.f;
        const float L = wave_sum(e * lsum);
        if (lane < 32) ldf[L_WTS + lane] = e / L;
        LGKM0();
        if (lane == 0) flg[F_W2] = 1u;
      }
      __builtin_amdgcn_s_setprio(0);
    }
  v4u GU[16];
  f32x4 gacc = {0.f, 0.f, 0.f, 0.f};
  unsigned xraw3 = 0u;
  if (t3a && wave == 1) {
    xraw3 = (unsigned)x[64 * rb + 4 * (lane & 15) + 2 * lb5 + lb4];
    while (flg[F_GU] < 16u) __builtin_amdgcn_s_sleep(4);
    CBAR();
#pragma unroll
    for (int t = 0; t < 16; ++t) GU[t] = *(const LAS v4u*)(lds + L_GU + t * 256 + lane * 4);
    const unsigned one2 = b3 ? 0x3f803f80u : 0u;
    const v4u A1 = {one2 & m0, one2 & m1, one2 & m2, one2 & m3};
#pragma unroll
    for (int t = 0; t < 16; ++t) gacc = mfma(A1, GU[t], gacc);
  }

  do_t2();
  if (t3a && wave == 1) {
    while (flg[F_YP] < 8u) __builtin_amdgcn_s_sleep(1);
    CBAR();
    float at;
    {
      float r[8];
#pragma unroll
      for (int i = 0; i < 8; ++i) r[i] = ldf[L_RED1 + i * 64 + lane];
      at = ((r[0] + r[1]) + (r[2] + r[3])) + ((r[4] + r[5]) + (r[6] + r[7]));
    }
    const float h = rbf(bflo(xraw3) + rbf(at));
    const unsigned hb = asu(h) >> 16;
    const unsigned hd = hb | (hb << 16);
    const unsigned n0 = b3 ? 0u : m0, n1 = b3 ? 0u : m1, n2 = b3 ? 0u : m2, n3 = b3 ? 0u : m3;
    const float piv = asf((unsigned)__builtin_amdgcn_readfirstlane((int)asu(h)));
    const float d = h - piv;
    float st = tr32(d, d * d);
#define T3A_STEP(t) { const unsigned hv = dppu<BCAST0 + (t)>(hd); const v4u A = {hv & n0, hv & n1, hv & n2, hv & n3}; gacc = mfma(A, GU[t], gacc); }
    T3A_STEP(0) T3A_STEP(1) st = tr16(st, st); T3A_STEP(2) T3A_STEP(3) st += dpp<XOR1>(st); T3A_STEP(4) T3A_STEP(5) st += dpp<XOR2>(st);
    T3A_STEP(6) T3A_STEP(7) st += dpp<ROR8>(st); T3A_STEP(8) T3A_STEP(9) st += dpp<ROR4>(st); T3A_STEP(10) T3A_STEP(11)
    const float s2 = asf((unsigned)__builtin_amdgcn_readlane((int)asu(st), 32));
    if (lane == 0) __builtin_amdgcn_raw_buffer_store_b128(v4u{asu(piv), asu(st), asu(s2), 0u}, rs, (int)(WS_ST + cb * 128 + rb * 16), 0, AUX_SC1);
    T3A_STEP(12) T3A_STEP(13) T3A_STEP(14) T3A_STEP(15)
#undef T3A_STEP
    __builtin_amdgcn_raw_buffer_store_b128(__builtin_bit_cast(v4u, gacc), rs, (int)(WS_G + (cb * 7 + rb) * 1024 + (lane & 7) * 128 + (4 * lb4 + 2 * lb5 + lb3) * 16), 0, AUX_SC1);
  }
  } else {
    const int v = wave - 8;
    __builtin_amdgcn_s_setprio(3);
    if (v < 7) {
      const int r0 = 112 * (j & 3) + 16 * v + (lane >> 3);
      const v4u xv = *(const v4u*)(x + 8 * (lane < 56 ? lane : 55));
      const unsigned xr0 = (unsigned)x[r0], xr1 = (unsigned)x[r0 + 8];
      const u16* wqp = wq + (size_t)r0 * 4096 + 64 * (8 * g + (j >> 2)) + 8 * (lane & 7);
      const v4u W0 = ldnt(wqp), W1 = ldnt(wqp + (size_t)8 * 4096);
      CBAR();
      __syncthreads();
      float xf[8];
#pragma unroll
      for (int i = 0; i < 4; ++i) { xf[2 * i] = bflo(xv[i]); xf[2 * i + 1] = bfhi(xv[i]); }
      float sm = ((xf[0] + xf[1]) + (xf[2] + xf[3])) + ((xf[4] + xf[5]) + (xf[6] + xf[7]));
      sm = lane < 56 ? sm : 0.f;
      const float mean = wave_sum(sm) * (1.f / 448.f);
      float vs = 0.f;
#pragma unroll
      for (int i = 0; i < 8; ++i) { const float d = xf[i] - mean; vs += d * d; }
      vs = lane < 56 ? vs : 0.f;
      const float var = wave_sum(vs) * (1.f / 448.f);
      const float mb = rbf(mean), den = rbf(__builtin_sqrtf(rbf(rbf(var) + 1e-5f)));
      const float xn0 = rbf(rbf(bflo(xr0) - mb) / den), xn1 = rbf(rbf(bflo(xr1) - mb) / den);
      float acc[8];
#pragma unroll
      for (int c = 0; c < 4; ++c) {
        acc[2 * c] = fmaf(xn1, bflo(W1[c]), xn0 * bflo(W0[c]));
        acc[2 * c + 1] = fmaf(xn1, bfhi(W1[c]), xn0 * bfhi(W0[c]));
      }
      const float qv = red8_b345(acc, b3);
      ldf[L_QP + v * 64 + 8 * (lane & 7) + 4 * lb5 + 2 * lb4 + lb3] = qv;
      LGKM0();
      unsigned oldq = 0;
      if (lane == 0) oldq = LDS_INC(F_QC);
      oldq = __builtin_amdgcn_readfirstlane(oldq);
      if (oldq == 6u) {
        while (flg[F_T] == 0u) __builtin_amdgcn_s_sleep(1);
        CBAR();
        const u64 T = ((u64)lds[L_TAG + 1] << 32) | lds[L_TAG];
        float qs = 0.f;
#pragma unroll
        for (int w = 0; w < 7; ++w) qs += ldf[L_QP + w * 64 + lane];
        const unsigned qbits = (asu(qs) + 0x8u) & 0xfffffff0u;
        v2u gr = {qbits | ((unsigned)T & 0xfu), (unsigned)(T >> 4)};
        __builtin_amdgcn_raw_buffer_store_b64(gr, rs, (int)(WS_QG + (8 * g + (j >> 2)) * 2048 + (j & 3) * 512 + lane * 8), 0, AUX_SC1);
        if (lane == 0) flg[F_QP] = 1u;
      }
    } else {
      const v2u ev = ld64(rs, (int)WS_E);
      const v4u e4 = {EMPTY, EMPTY, EMPTY, EMPTY};
      __builtin_amdgcn_raw_buffer_store_b128(e4, rs, (int)(WS_PART + ((2 * g) * 32 + j) * 1024 + 16 * lane), 0, AUX_SC1);
      __builtin_amdgcn_raw_buffer_store_b128(e4, rs, (int)(WS_PART + ((2 * g + 1) * 32 + j) * 1024 + 16 * lane), 0, AUX_SC1);
      if (lane < 2) __builtin_amdgcn_raw_buffer_store_b64(v2u{EMPTY, EMPTY}, rs, (int)(WS_ML + ((2 * g + lane) * 32 + j) * 8), 0, AUX_SC1);
      if (t2 && lane < 16) __builtin_amdgcn_raw_buffer_store_b128(e4, rs, (int)(WS_Y + (cb7 * 32 + hq * 2 + rh) * 256 + 16 * lane), 0, AUX_SC1);
      if (t3a) {
        if (lane == 0) __builtin_amdgcn_raw_buffer_store_b128(e4, rs, (int)(WS_ST + cb * 128 + rb * 16), 0, AUX_SC1);
        __builtin_amdgcn_raw_buffer_store_b128(e4, rs, (int)(WS_G + (cb * 7 + rb) * 1024 + 16 * lane), 0, AUX_SC1);
      }
      if (t3b) {
        __builtin_amdgcn_raw_buffer_store_b128(e4, rs, (int)(WS_F + fu * 1792 + 16 * lane), 0, AUX_SC1);
        if (lane < 48) __builtin_amdgcn_raw_buffer_store_b128(e4, rs, (int)(WS_F + fu * 1792 + 1024 + 16 * lane), 0, AUX_SC1);
      }
      CBAR();
      __syncthreads();
      VM0();
      const u64 T = (((u64)ev[1] << 32) | ev[0]) + 1ull;
      if (lane == 0) {
        __builtin_amdgcn_raw_buffer_store_b64(v2u{(unsigned)T, (unsigned)(T >> 32)}, rs, (int)(WS_READY + blockIdx.x * 8), 0, AUX_SC1);
        lds[L_TAG] = (unsigned)T; lds[L_TAG + 1] = (unsigned)(T >> 32);
        LGKM0();
        flg[F_T] = 1u;
      }
    }
    __builtin_amdgcn_s_setprio(0);
    const u16* vb = vc + ((size_t)g * 8192 + 256 * j + 32 * v + (lane >> 4)) * 256 + 8 * (lane & 15);
    v4u V[16];
    while (flg[F_KI0 + v] == 0u) __builtin_amdgcn_s_sleep(1);
    CBAR();
    if (t2) wo_r = ldnt(wo_p);
    if (t3a) wgu_r = ldnt(wgu_p);
    CBAR();
#pragma unroll
    for (int i = 0; i < 16; ++i) { V[i] = ldnt(vb + (i >> 1) * 1024 + (i & 1) * 128); CBAR(); }
    __builtin_amdgcn_s_setprio(2);
    if (t3a) {
      *(LAS v4u*)(lds + L_GU + wave * 256 + lane * 4) = wgu_r;
      LGKM0();
      if (lane == 0) LDS_INC(F_GU);
    }
    while (flg[F_P0 + v] == 0u) __builtin_amdgcn_s_sleep(2);
    CBAR();
    const LAS v4u* pp = (const LAS v4u*)(lds + L_P + v * 64 + lb3 * 32 + (lane >> 4) * 8);
    const v4u pa = pp[0], pb = pp[1];
    f32x4 a0 = {0.f, 0.f, 0.f, 0.f}, a1 = {0.f, 0.f, 0.f, 0.f};
#pragma unroll
    for (int kg = 0; kg < 8; ++kg) {
      const unsigned pd = kg < 4 ? pa[kg & 3] : pb[kg & 3];
      const v4u A = {pd & m0, pd & m1, pd & m2, pd & m3};
      a0 = mfma(A, V[2 * kg], a0);
      a1 = mfma(A, V[2 * kg + 1], a1);
    }
    LAS f32x4* rp = (LAS f32x4*)(ldf + L_RED + v * 512 + (lane >> 5) * 256 + 8 * (lane & 15) + 4 * lb4);
    rp[0] = a0; rp[32] = a1;
    LGKM0();
    unsigned oldv = 0;
    if (lane == 0) oldv = LDS_INC(F_VC);
    oldv = __builtin_amdgcn_readfirstlane(oldv);
    if (oldv == 7u) {
      CBAR();
      const LAS f32x4* sp = (const LAS f32x4*)(ldf + L_RED + 4 * lane);
      f32x4 r0[8], r1[8];
#pragma unroll
      for (int w = 0; w < 8; ++w) r0[w] = sp[w * 128];
#pragma unroll
      for (int w = 0; w < 8; ++w) r1[w] = sp[w * 128 + 64];
      const f32x4 s0 = ((r0[0] + r0[1]) + (r0[2] + r0[3])) + ((r0[4] + r0[5]) + (r0[6] + r0[7]));
      __builtin_amdgcn_raw_buffer_store_b128(__builtin_bit_cast(v4u, s0), rs, (int)(WS_PART + ((2 * g) * 32 + j) * 1024 + 16 * lane), 0, AUX_SC1);
      const f32x4 s1 = ((r1[0] + r1[1]) + (r1[2] + r1[3])) + ((r1[4] + r1[5]) + (r1[6] + r1[7]));
      __builtin_amdgcn_raw_buffer_store_b128(__builtin_bit_cast(v4u, s1), rs, (int)(WS_PART + ((2 * g + 1) * 32 + j) * 1024 + 16 * lane), 0, AUX_SC1);
    }
    do_t2();
    if (t3a) {
      v4u yv;
      unsigned spins = 0;
      bool ok = false;
      int off = (int)(WS_Y + (rb * 32 + 4 * v + (lane >> 4)) * 256 + (lane & 15) * 16);
      while (flg[F_R] == 0u) __builtin_amdgcn_s_sleep(1);
      __builtin_amdgcn_s_sleep(T3DELAY);
      for (;;) {
        OPAQUE(off);
        if (!ok) { yv = ld128(rs, off); ok = full4(yv); }
        if (__all(ok) || ++spins > SPIN_CAP) break;
        __builtin_amdgcn_s_sleep(PSLEEP);
      }
      const float u0 = tr32(asf(yv[0]), asf(yv[2])), u1 = tr32(asf(yv[1]), asf(yv[3]));
      ldf[L_RED1 + v * 64 + lane] = tr16(u0, u1);
      LGKM0();
      if (lane == 0) LDS_INC(F_YP);
    }
  if (t3b && v == 4) {
    while (flg[F_VC] < 8u || flg[F_R] == 0u) __builtin_amdgcn_s_sleep(2);
    v4u yv = {0u, 0u, 0u, 0u};
    bool ok = lane >= 32;
    unsigned spins = 0;
    int off = (int)(WS_Y + ((fu % 7) * 32 + (lane & 31)) * 256);
    for (;;) {
      OPAQUE(off);
      if (!ok) { yv = ld128(rs, off); ok = full4(yv); }
      if (__all(ok) || ++spins > SPIN_CAP) break;
      __builtin_amdgcn_s_sleep(4);
    }
    if (lane == 0) flg[F_GP] = 1u;
  }
  if (t3b && v < 4) {
    if (!t2) { while (flg[F_VC] < 8u) __builtin_amdgcn_s_sleep(1); }
    CBAR();
    v4u wd_r[8];
    {
      const int col = 128 * v + 8 * (lane & 15);
      const u16* wp = wd + (size_t)(32 * fu + (lane >> 4)) * 448 + (col < 448 ? col : col - 64);
#pragma unroll
      for (int i = 0; i < 8; ++i) wd_r[i] = ldnt(wp + (size_t)(4 * i) * 448);
    }
    CBAR();
    while (flg[F_GP] == 0u) __builtin_amdgcn_s_sleep(1);
    __builtin_amdgcn_s_sleep(T3BDELAY);
    v4u gv = {0u, 0u, 0u, 0u};
    bool ok = lane == 63;
    unsigned spins = 0;
    int off = lane < 56 ? (int)(WS_G + ((fu >> 1) * 7 + (lane >> 3)) * 1024 + (4 * (fu & 1) + v) * 128 + (lane & 7) * 16)
                        : (int)(WS_ST + (fu >> 1) * 128 + (lane & 7) * 16);
    for (;;) {
      OPAQUE(off);
      if (!ok) { gv = ld128(rs, off); ok = full4(gv); }
      if (__all(ok) || ++spins > SPIN_CAP) break;
      __builtin_amdgcn_s_sleep(P3BSLEEP);
    }
    const bool live = lane < 56;
    const float piv = asf(gv[0]), s1 = asf(gv[1]), s2 = asf(gv[2]);
    const float dm = s1 * (1.f / 64.f);
    const float mw = lane == 63 ? 0.f : piv + dm, mq = s2 - s1 * dm;
    float ms = mw;
    ms += dpp<XOR1>(ms); ms += dpp<XOR2>(ms); ms += dpp<HMIR>(ms);
    float a0 = live ? asf(gv[0]) : 0.f, a1 = live ? asf(gv[1]) : 0.f, a2 = live ? asf(gv[2]) : 0.f, a3 = live ? asf(gv[3]) : 0.f;
    a0 = tr32(a0, a0); a1 = tr32(a1, a1); a2 = tr32(a2, a2); a3 = tr32(a3, a3);
    const float mean = asf((unsigned)__builtin_amdgcn_readlane((int)asu(ms), 56)) * (1.f / 7.f);
    const float dd = mw - mean;
    float vv = lane == 63 ? 0.f : mq + 64.f * dd * dd;
    a0 = tr16(a0, a0); a1 = tr16(a1, a1); a2 = tr16(a2, a2); a3 = tr16(a3, a3);
    vv += dpp<XOR1>(vv); vv += dpp<XOR2>(vv); vv += dpp<HMIR>(vv);
    a0 += dpp<ROR8>(a0); a1 += dpp<ROR8>(a1); a2 += dpp<ROR8>(a2); a3 += dpp<ROR8>(a3);
    const float var = fmaxf(asf((unsigned)__builtin_amdgcn_readlane((int)asu(vv), 56)) * (1.f / 448.f), 0.f);
    const float mb = rbf(mean), den = rbf(__builtin_amdgcn_sqrtf(rbf(rbf(var) + 1e-5f)));
    const float rden = __builtin_amdgcn_rcpf(den);
    const int gq = lane >> 4;
    const float aq = gq == 0 ? a0 : gq == 1 ? a1 : gq == 2 ? a2 : a3;
    const float z = rbf((aq - mb * dpp<XOR2>(aq)) * rden);
    const float u = dpp<XOR1>(z);
    const float tt = 0.7978845608028654f * (z + 0.044715f * z * z * z);
    const float e2 = __builtin_amdgcn_exp2f(tt * 2.8853900817779268f);
    const float th = 1.f - 2.f * __builtin_amdgcn_rcpf(e2 + 1.f);
    const unsigned ad = bfdup(rbf(0.5f * z * (1.f + th)) * u);
    if ((lane & 11) == 0) lds[L_AV + gq * 8 + 2 * v + ((lane >> 2) & 1)] = ad;
    LGKM0();
    if (lane == 0) LDS_INC(F_AV);
    while (flg[F_AV] < 4u) ;
    CBAR();
    const v4u av0 = *(const LAS v4u*)(lds + L_AV + gq * 8), av1 = *(const LAS v4u*)(lds + L_AV + gq * 8 + 4);
    const unsigned n0 = b3 ? 0u : m0, n1 = b3 ? 0u : m1, n2 = b3 ? 0u : m2, n3 = b3 ? 0u : m3;
    f32x4 f0 = {0.f, 0.f, 0.f, 0.f}, f1 = {0.f, 0.f, 0.f, 0.f};
#pragma unroll
    for (int i = 0; i < 4; ++i) {
      const v4u A0 = {av0[i] & n0, av0[i] & n1, av0[i] & n2, av0[i] & n3}, A1 = {av1[i] & n0, av1[i] & n1, av1[i] & n2, av1[i] & n3};
      f0 = mfma(A0, wd_r[i], f0); f1 = mfma(A1, wd_r[i + 4], f1);
    }
    f0 += f1;
    const int c0 = 128 * v + 8 * (lane & 15) + 4 * lb4;
    if (lane < 32 && c0 < 448) __builtin_amdgcn_raw_buffer_store_b128(__builtin_bit_cast(v4u, f0), rs, (int)(WS_F + fu * 1792 + c0 * 4), 0, AUX_SC1);
  }
  }
  if (t4 && wave < 6) {
    const int rid = j;
    const int frow = 16 * wave + (lane >> 2);
    int off = wave < 4 ? (int)(WS_F + (frow < 56 ? frow : 0) * 1792 + (16 * rid + 4 * (lane & 3)) * 4)
                       : (int)(WS_Y + ((rid >> 2) * 32 + 16 * (wave - 4) + (lane >> 2)) * 256 + (16 * (rid & 3) + 4 * (lane & 3)) * 4);
    const unsigned xraw = (unsigned)x[16 * rid + (lane & 15)];
    v4u pv = {0u, 0u, 0u, 0u};
    unsigned spins = 0;
    bool ok = wave < 4 && frow >= 56;
    if (wave < 4) { while (flg[F_Y4] < 2u) __builtin_amdgcn_s_sleep(1); __builtin_amdgcn_s_sleep(T4DELAY); }
    else { while (flg[F_R] == 0u) __builtin_amdgcn_s_sleep(1); __builtin_amdgcn_s_sleep(T3DELAY); }
    for (;;) {
      OPAQUE(off);
      if (!ok) { pv = ld128(rs, off); ok = full4(pv); }
      if (__all(ok) || ++spins > SPIN_CAP) break;
      __builtin_amdgcn_s_sleep(P4SLEEP);
    }
    const float a0 = tr32(asf(pv[0]), asf(pv[2])), a1 = tr32(asf(pv[1]), asf(pv[3]));
    float z = tr16(a0, a1);
    z += dpp<ROR8>(z); z += dpp<ROR4>(z);
    const int col = 4 * (lane & 3) + 2 * lb5 + lb4;
    if ((lane & 12) == 0) ldf[L_RED4 + wave * 16 + col] = z;
    LGKM0();
    unsigned oldf = 0;
    if (lane == 0) oldf = LDS_INC(F_Y4);
    oldf = __builtin_amdgcn_readfirstlane(oldf);
    if (oldf == 5u) {
      CBAR();
      if (lane < 16) {
        const float f = (ldf[L_RED4 + lane] + ldf[L_RED4 + 16 + lane]) + (ldf[L_RED4 + 32 + lane] + ldf[L_RED4 + 48 + lane]);
        const float h = rbf(bflo(xraw) + rbf(ldf[L_RED4 + 64 + lane] + ldf[L_RED4 + 80 + lane]));
        const unsigned ob = f2bf(h + rbf(f));
        const unsigned pk = ob | (dppu<XOR1>(ob) << 16);
        const __amdgpu_buffer_rsrc_t ro = __builtin_amdgcn_make_buffer_rsrc((void*)out, 0, 896, 0x00020000);
        if ((lane & 1) == 0) __builtin_amdgcn_raw_buffer_store_b32(pk, ro, (16 * rid + lane) * 2, 0, AUX_SC1);
      }
    }
  }
}

extern "C" void kernel_launch(void* const* d_in, const int* in_sizes, int n_in,
                              void* d_out, int out_size, void* d_ws, size_t ws_size,
                              hipStream_t stream) {
  fused_decode<<<256, 1024, 0, stream>>>((const u16*)d_in[0], (const u16*)d_in[1], (const u16*)d_in[2], (const u16*)d_in[3],
                                         (const u16*)d_in[4], (const u16*)d_in[6], (const u16*)d_in[7], (const u16*)d_in[8],
                                         (u16*)d_out, (unsigned char*)d_ws);
}
```
